```python
import torch

def _softmax(x, axis=-1):
    return torch.softmax(x, dim=axis)
def _gelu(x):
    return torch.nn.functional.gelu(x, approximate="tanh")
def _silu(x):
    return torch.nn.functional.silu(x)
def _sigmoid(x):
    return torch.sigmoid(x)
def _layernorm(x, axis=-1, eps=1e-5):
    m = x.mean(dim=axis, keepdim=True)
    v = x.var(dim=axis, keepdim=True, unbiased=False)
    return (x - m) / torch.sqrt(v + eps)
def _scatter_add(tgt, idx, src, axis=0):
    return tgt.clone().index_add_(axis, idx, src)
def _segment_sum(src, seg, n):
    out = torch.zeros((n,) + src.shape[1:], dtype=src.dtype)
    return out.index_add_(0, seg, src)
def _pad(x, pw):
    flat = tuple(v for pair in reversed(pw) for v in pair)
    return torch.nn.functional.pad(x, flat)

kernel_name = 'recurrent_scan__compressed_kv_cla__dense_activation_silu__decode'

def setup_inputs():

    _w = (lambda a: (lambda t: t.to(torch.bfloat16) if t.is_floating_point() else t)(torch.as_tensor(a)))
    def _A_setup():
        B, H, Sq, Sk, D, M = 1, 32, 1, 32768, 128, 512
        Dc = 4 * D
        return {
            "x": _w((lambda *s: torch.randn(*s, dtype=torch.bfloat16) * 0.1)(B, Sq, M)),
            "c_kv": _w((lambda *s: torch.randn(*s, dtype=torch.bfloat16) * 3.2)(B, Sk, Dc)),
            "w_q": _w((lambda *s: torch.randn(*s, dtype=torch.bfloat16) * 0.8)(M, H, D) * (M ** -0.5)),
            "w_uk": _w((lambda *s: torch.randn(*s, dtype=torch.bfloat16) * 0.8)(Dc, H, D) * (Dc ** -0.5)),
            "w_uv": _w((lambda *s: torch.randn(*s, dtype=torch.bfloat16) * 3.2)(Dc, H, D) * (Dc ** -0.5)),
            "w_o": _w((lambda *s: torch.randn(*s, dtype=torch.bfloat16) * 3.2)(H, D, M) * ((H * D) ** -0.5)),
        }
    _A_in = _A_setup()

    def _B_setup():
        B, S, D, F = 1, 1, 512, 1376
        return {
            "x": _w((lambda *s: torch.randn(*s, dtype=torch.bfloat16) * 0.1)(B, S, D)),
            "w1": _w((lambda *s: torch.randn(*s, dtype=torch.bfloat16) * 3.2)(D, F) * (D ** -0.5)),
            "w2": _w((lambda *s: torch.randn(*s, dtype=torch.bfloat16) * 0.8)(F, D) * (F ** -0.5)),
        }
    _B_in = _B_setup()

    return {
        "A_x": _A_in["x"],
        "A_c_kv": _A_in["c_kv"],
        "A_w_q": _A_in["w_q"],
        "A_w_uk": _A_in["w_uk"],
        "A_w_uv": _A_in["w_uv"],
        "A_w_o": _A_in["w_o"],
        "B_x": _B_in["x"],
        "B_w1": _B_in["w1"],
        "B_w2": _B_in["w2"],
    }

def reference(A_x, A_c_kv, A_w_q, A_w_uk, A_w_uv, A_w_o, B_x, B_w1, B_w2):
    def _A_ref(x, c_kv, w_q, w_uk, w_uv, w_o):
        B, Sq, M = x.shape[0], x.shape[1], x.shape[2]
        Sk = c_kv.shape[1]
        H, D = w_q.shape[1], w_q.shape[2]
        q = torch.einsum("bqm,mhd->bhqd", x, w_q)
        k = torch.einsum("bkc,chd->bhkd", c_kv, w_uk)
        v = torch.einsum("bkc,chd->bhkd", c_kv, w_uv)
        scores = torch.einsum("bhqd,bhkd->bhqk", q, k) * (D ** -0.5)
        off = Sk - Sq
        qi = torch.arange(Sq)[:, None]
        kj = torch.arange(Sk)[None, :]
        cmask = kj <= (qi + off)
        scores = torch.where(cmask[None, None], scores, -1e9)
        p = _softmax(scores, axis=-1)
        ctx = torch.einsum("bhqk,bhkd->bhqd", p, v)
        return torch.einsum("bhqd,hdm->bqm", ctx, w_o)

    def _B_ref(x, w1, w2):
        h = torch.einsum("bsd,df->bsf", x, w1)
        h = _silu(h)
        out = torch.einsum("bsf,fd->bsd", h, w2)
        return out

    x = A_x
    xn = _layernorm(x, axis=-1)
    s = _A_ref(xn, A_c_kv, A_w_q, A_w_uk, A_w_uv, A_w_o)
    g = _silu(_B_ref(xn, B_w1, B_w2))
    return x + s * g
```

```cpp
#include <hip/hip_runtime.h>
#include <hip/hip_bf16.h>
#include <stdint.h>

typedef unsigned short u16;
constexpr int H = 32, D = 128, M = 512, DC = 512, SK = 32768, F = 1376;
__device__ __forceinline__ float bf2f(u16 b) { return __uint_as_float(((unsigned)b) << 16); }
__device__ __forceinline__ u16 f2bf(float f) { unsigned u = __float_as_uint(f); u += 0x7fffu + ((u >> 16) & 1u); return (u16)(u >> 16); }
__device__ __forceinline__ float rbf(float f) { return bf2f(f2bf(f)); }

__device__ void layernorm_to_lds(const u16* x, float* xn, float* red) {
    int t = threadIdx.x;
    float a = bf2f(x[t]), b = bf2f(x[t + 256]);
    float s = a + b;
    for (int o = 32; o >= 1; o >>= 1) s += __shfl_xor(s, o);
    if ((t & 63) == 0) red[t >> 6] = s;
    __syncthreads();
    float mean = (red[0] + red[1] + red[2] + red[3]) * (1.0f / 512);
    __syncthreads();
    float da = a - mean, db = b - mean;
    float v = da * da + db * db;
    for (int o = 32; o >= 1; o >>= 1) v += __shfl_xor(v, o);
    if ((t & 63) == 0) red[t >> 6] = v;
    __syncthreads();
    float var = (red[0] + red[1] + red[2] + red[3]) * (1.0f / 512);
    float r = 1.0f / sqrtf(var + 1e-5f);
    xn[t] = rbf(da * r); xn[t + 256] = rbf(db * r);
    __syncthreads();
}

__global__ __launch_bounds__(256) void k1(const u16* x, const u16* wq, const u16* wuk, const u16* w1, float* qabs, float* hid) {
    __shared__ float xn[512]; __shared__ float red[8]; __shared__ float q[2][128];
    layernorm_to_lds(x, xn, red);
    int h = blockIdx.x >> 3, s = blockIdx.x & 7, t = threadIdx.x;
    int d = t & 127, half = t >> 7;
    float acc = 0.f;
    for (int m = half * 256; m < half * 256 + 256; ++m) acc += xn[m] * bf2f(wq[(size_t)m * 4096 + h * 128 + d]);
    q[half][d] = acc;
    __syncthreads();
    int c = s * 64 + (t >> 2), part = t & 3;
    float a2 = 0.f;
    for (int dd = part * 32; dd < part * 32 + 32; ++dd) a2 += (q[0][dd] + q[1][dd]) * bf2f(wuk[(size_t)c * 4096 + h * 128 + dd]);
    a2 += __shfl_xor(a2, 1); a2 += __shfl_xor(a2, 2);
    if (part == 0) qabs[h * 512 + c] = a2 * 0.08838834764831845f;
    int f = blockIdx.x * 6 + (t >> 5);
    if ((t >> 5) < 6 && f < F) {
        float a3 = 0.f;
        for (int m = (t & 31); m < 512; m += 32) a3 += xn[m] * bf2f(w1[(size_t)m * F + f]);
        for (int o = 16; o >= 1; o >>= 1) a3 += __shfl_xor(a3, o);
        if ((t & 31) == 0) hid[f] = a3 / (1.0f + expf(-a3));
    }
}

__global__ __launch_bounds__(256) void k2(const u16* ckv, const float* qabs, float* part, float* ml) {
    __shared__ float sc[32][129];
    int b = blockIdx.x, t = threadIdx.x;
    const u16* base = ckv + (size_t)b * 128 * 512;
    int k = t & 127, hh = t >> 7;
    for (int h = hh * 16; h < hh * 16 + 16; ++h) {
        float a = 0.f;
        for (int c = 0; c < 512; ++c) a += qabs[h * 512 + c] * bf2f(base[k * 512 + c]);
        sc[h][k] = a;
    }
    __syncthreads();
    if (t < 32) {
        float m = -1e30f;
        for (int kk = 0; kk < 128; ++kk) m = fmaxf(m, sc[t][kk]);
        float l = 0.f;
        for (int kk = 0; kk < 128; ++kk) { float p = expf(sc[t][kk] - m); sc[t][kk] = p; l += p; }
        ml[(b * 32 + t) * 2] = m; ml[(b * 32 + t) * 2 + 1] = l;
    }
    __syncthreads();
    for (int c = t; c < 512; c += 256) {
        float acc[32];
        for (int h = 0; h < 32; ++h) acc[h] = 0.f;
        for (int kk = 0; kk < 128; ++kk) { float v = bf2f(base[kk * 512 + c]);
#pragma unroll
            for (int h = 0; h < 32; ++h) acc[h] += sc[h][kk] * v; }
#pragma unroll
        for (int h = 0; h < 32; ++h) part[((size_t)b * 32 + h) * 512 + c] = acc[h];
    }
}

__global__ __launch_bounds__(256) void k3(const float* part, const float* ml, const u16* wuv, const u16* wo, float* spart) {
    __shared__ float wgt[256]; __shared__ float cc[512]; __shared__ float ctx[2][128]; __shared__ float red[8];
    int h = blockIdx.x, t = threadIdx.x;
    float m = ml[(t * 32 + h) * 2], l = ml[(t * 32 + h) * 2 + 1];
    float mm = m;
    for (int o = 32; o >= 1; o >>= 1) mm = fmaxf(mm, __shfl_xor(mm, o));
    if ((t & 63) == 0) red[t >> 6] = mm;
    __syncthreads();
    mm = fmaxf(fmaxf(red[0], red[1]), fmaxf(red[2], red[3]));
    __syncthreads();
    float w = expf(m - mm); float ls = l * w;
    for (int o = 32; o >= 1; o >>= 1) ls += __shfl_xor(ls, o);
    if ((t & 63) == 0) red[t >> 6] = ls;
    __syncthreads();
    float L = red[0] + red[1] + red[2] + red[3];
    wgt[t] = w / L;
    __syncthreads();
    for (int c = t; c < 512; c += 256) { float a = 0.f; for (int b = 0; b < 256; ++b) a += wgt[b] * part[((size_t)b * 32 + h) * 512 + c]; cc[c] = a; }
    __syncthreads();
    int d = t & 127, half = t >> 7; float a = 0.f;
    for (int c = half * 256; c < half * 256 + 256; ++c) a += cc[c] * bf2f(wuv[(size_t)c * 4096 + h * 128 + d]);
    ctx[half][d] = a;
    __syncthreads();
    for (int mo = t; mo < 512; mo += 256) { float s = 0.f; for (int dd = 0; dd < 128; ++dd) s += (ctx[0][dd] + ctx[1][dd]) * bf2f(wo[((size_t)h * 128 + dd) * 512 + mo]); spart[h * 512 + mo] = s; }
}

__global__ __launch_bounds__(512) void k4(const u16* x, const float* spart, const float* hid, const u16* w2, u16* out) {
    int mo = threadIdx.x;
    float s = 0.f; for (int h = 0; h < 32; ++h) s += spart[h * 512 + mo];
    float o2 = 0.f; for (int f = 0; f < F; ++f) o2 += hid[f] * bf2f(w2[(size_t)f * 512 + mo]);
    float g = o2 / (1.0f + expf(-o2));
    out[mo] = f2bf(bf2f(x[mo]) + s * g);
}

extern "C" void kernel_launch(void* const* d_in, const int* in_sizes, int n_in, void* d_out, int out_size, void* d_ws, size_t ws_size, hipStream_t stream) {
    const u16* x = (const u16*)d_in[0]; const u16* ckv = (const u16*)d_in[1]; const u16* wq = (const u16*)d_in[2]; const u16* wuk = (const u16*)d_in[3];
    const u16* wuv = (const u16*)d_in[4]; const u16* wo = (const u16*)d_in[5]; const u16* w1 = (const u16*)d_in[7]; const u16* w2 = (const u16*)d_in[8];
    float* ws = (float*)d_ws;
    float* qabs = ws; float* hid = ws + 16384; float* ml = ws + 32768; float* spart = ws + 65536; float* part = ws + 131072;
    k1<<<256, 256, 0, stream>>>(x, wq, wuk, w1, qabs, hid);
    k2<<<256, 256, 0, stream>>>(ckv, qabs, part, ml);
    k3<<<32, 256, 0, stream>>>(part, ml, wuv, wo, spart);
    k4<<<1, 512, 0, stream>>>(x, spart, hid, w2, (u16*)d_out);
}
```

```cpp
#include <hip/hip_runtime.h>
#include <hip/hip_bf16.h>
#include <stdint.h>

typedef unsigned short u16;
constexpr int H = 32, D = 128, M = 512, DC = 512, SK = 32768, F = 1376;
__device__ __forceinline__ float bf2f(u16 b) { return __uint_as_float(((unsigned)b) << 16); }
__device__ __forceinline__ u16 f2bf(float f) { unsigned u = __float_as_uint(f); u += 0x7fffu + ((u >> 16) & 1u); return (u16)(u >> 16); }
__device__ __forceinline__ float rbf(float f) { return bf2f(f2bf(f)); }

__device__ void layernorm_to_lds(const u16* x, float* xn, float* red) {
    int t = threadIdx.x;
    float a = bf2f(x[t]), b = bf2f(x[t + 256]);
    float s = a + b;
    for (int o = 32; o >= 1; o >>= 1) s += __shfl_xor(s, o);
    if ((t & 63) == 0) red[t >> 6] = s;
    __syncthreads();
    float mean = (red[0] + red[1] + red[2] + red[3]) * (1.0f / 512);
    __syncthreads();
    float da = a - mean, db = b - mean;
    float v = da * da + db * db;
    for (int o = 32; o >= 1; o >>= 1) v += __shfl_xor(v, o);
    if ((t & 63) == 0) red[t >> 6] = v;
    __syncthreads();
    float var = (red[0] + red[1] + red[2] + red[3]) * (1.0f / 512);
    float r = 1.0f / sqrtf(var + 1e-5f);
    xn[t] = rbf(da * r); xn[t + 256] = rbf(db * r);
    __syncthreads();
}

__global__ __launch_bounds__(256) void k1(const u16* x, const u16* wq, const u16* wuk, const u16* w1, float* qabs, float* hid) {
    __shared__ float xn[512]; __shared__ float red[8]; __shared__ float q[2][128];
    layernorm_to_lds(x, xn, red);
    int h = blockIdx.x >> 3, s = blockIdx.x & 7, t = threadIdx.x;
    int d = t & 127, half = t >> 7;
    float acc = 0.f;
    for (int m = half * 256; m < half * 256 + 256; ++m) acc += xn[m] * bf2f(wq[(size_t)m * 4096 + h * 128 + d]);
    q[half][d] = acc;
    __syncthreads();
    int c = s * 64 + (t >> 2), part = t & 3;
    float a2 = 0.f;
    for (int dd = part * 32; dd < part * 32 + 32; ++dd) a2 += (q[0][dd] + q[1][dd]) * bf2f(wuk[(size_t)c * 4096 + h * 128 + dd]);
    a2 += __shfl_xor(a2, 1); a2 += __shfl_xor(a2, 2);
    if (part == 0) qabs[h * 512 + c] = a2 * 0.08838834764831845f;
    int f = blockIdx.x * 6 + (t >> 5);
    if ((t >> 5) < 6 && f < F) {
        float a3 = 0.f;
        for (int m = (t & 31); m < 512; m += 32) a3 += xn[m] * bf2f(w1[(size_t)m * F + f]);
        for (int o = 16; o >= 1; o >>= 1) a3 += __shfl_xor(a3, o);
        if ((t & 31) == 0) hid[f] = a3 / (1.0f + expf(-a3));
    }
}

__global__ __launch_bounds__(256) void k2(const u16* ckv, const float* qabs, float* part, float* ml) {
    __shared__ float sc[32][129];
    int b = blockIdx.x, t = threadIdx.x;
    const u16* base = ckv + (size_t)b * 128 * 512;
    int k = t & 127, hh = t >> 7;
    for (int h = hh * 16; h < hh * 16 + 16; ++h) {
        float a = 0.f;
        for (int c = 0; c < 512; ++c) a += qabs[h * 512 + c] * bf2f(base[k * 512 + c]);
        sc[h][k] = a;
    }
    __syncthreads();
    if (t < 32) {
        float m = -1e30f;
        for (int kk = 0; kk < 128; ++kk) m = fmaxf(m, sc[t][kk]);
        float l = 0.f;
        for (int kk = 0; kk < 128; ++kk) { float p = expf(sc[t][kk] - m); sc[t][kk] = p; l += p; }
        ml[(b * 32 + t) * 2] = m; ml[(b * 32 + t) * 2 + 1] = l;
    }
    __syncthreads();
    for (int c = t; c < 512; c += 256) {
        float acc[32];
        for (int h = 0; h < 32; ++h) acc[h] = 0.f;
        for (int kk = 0; kk < 128; ++kk) { float v = bf2f(base[kk * 512 + c]);
#pragma unroll
            for (int h = 0; h < 32; ++h) acc[h] += sc[h][kk] * v; }
#pragma unroll
        for (int h = 0; h < 32; ++h) part[((size_t)b * 32 + h) * 512 + c] = acc[h];
    }
}

__global__ __launch_bounds__(256) void k3(const float* part, const float* ml, const u16* wuv, const u16* wo, float* spart) {
    __shared__ float wgt[256]; __shared__ float cc[512]; __shared__ float ctx[2][128]; __shared__ float red[8];
    int h = blockIdx.x, t = threadIdx.x;
    float m = ml[(t * 32 + h) * 2], l = ml[(t * 32 + h) * 2 + 1];
    float mm = m;
    for (int o = 32; o >= 1; o >>= 1) mm = fmaxf(mm, __shfl_xor(mm, o));
    if ((t & 63) == 0) red[t >> 6] = mm;
    __syncthreads();
    mm = fmaxf(fmaxf(red[0], red[1]), fmaxf(red[2], red[3]));
    __syncthreads();
    float w = expf(m - mm); float ls = l * w;
    for (int o = 32; o >= 1; o >>= 1) ls += __shfl_xor(ls, o);
    if ((t & 63) == 0) red[t >> 6] = ls;
    __syncthreads();
    float L = red[0] + red[1] + red[2] + red[3];
    wgt[t] = w / L;
    __syncthreads();
    for (int c = t; c < 512; c += 256) { float a = 0.f; for (int b = 0; b < 256; ++b) a += wgt[b] * part[((size_t)b * 32 + h) * 512 + c]; cc[c] = a; }
    __syncthreads();
    int d = t & 127, half = t >> 7; float a = 0.f;
    for (int c = half * 256; c < half * 256 + 256; ++c) a += cc[c] * bf2f(wuv[(size_t)c * 4096 + h * 128 + d]);
    ctx[half][d] = a;
    __syncthreads();
    for (int mo = t; mo < 512; mo += 256) { float s = 0.f; for (int dd = 0; dd < 128; ++dd) s += (ctx[0][dd] + ctx[1][dd]) * bf2f(wo[((size_t)h * 128 + dd) * 512 + mo]); spart[h * 512 + mo] = s; }
}

__global__ __launch_bounds__(512) void k4(const u16* x, const float* spart, const float* hid, const u16* w2, u16* out) {
    int mo = threadIdx.x;
    float s = 0.f; for (int h = 0; h < 32; ++h) s += spart[h * 512 + mo];
    float o2 = 0.f; for (int f = 0; f < F; ++f) o2 += hid[f] * bf2f(w2[(size_t)f * 512 + mo]);
    float g = o2 / (1.0f + expf(-o2));
    out[mo] = f2bf(bf2f(x[mo]) + s * g);
}

#define P_AUX 0
#define P_WIN 32
#define P_WHAT 1

#define GAS __attribute__((address_space(1)))
#define LAS __attribute__((address_space(3)))
__device__ __forceinline__ unsigned long long rt() { return __builtin_amdgcn_s_memrealtime(); }
__global__ __launch_bounds__(64) void kprobe(const char* src, unsigned long long* st) {
    __shared__ __attribute__((aligned(16))) char smem[131072];
    const unsigned long long t0 = rt();
    const int lane = threadIdx.x & 63, b = blockIdx.x;
    const char* base = src + (size_t)b * 131072;
#pragma unroll
    for (int i = 0; i < 128; ++i) {
        __builtin_amdgcn_global_load_lds((const GAS void*)(base + i * 1024 + lane * 16), (LAS void*)(smem + i * 1024), 16, 0, P_AUX);
        if (i >= P_WIN) asm volatile("s_waitcnt vmcnt(%0)" :: "n"(P_WIN));
    }
    asm volatile("s_waitcnt vmcnt(0)" ::: "memory");
    const unsigned long long t1 = rt();
    if (lane == 0) { st[b * 2] = t0; st[b * 2 + 1] = t1; }
}
__global__ __launch_bounds__(256) void kspin(const unsigned long long* st) {
    __shared__ unsigned long long a[256], c[256];
    const int t = threadIdx.x;
    a[t] = st[2 * t]; c[t] = st[2 * t + 1];
    __syncthreads();
    if (t == 0) {
        unsigned long long mn = ~0ull, mxs = 0, mxe = 0, own = 0;
        for (int i = 0; i < 256; ++i) { mn = a[i] < mn ? a[i] : mn; mxs = a[i] > mxs ? a[i] : mxs; mxe = c[i] > mxe ? c[i] : mxe; own = c[i] - a[i] > own ? c[i] - a[i] : own; }
        unsigned long long v = P_WHAT == 0 ? own : (P_WHAT == 1 ? mxs - mn : mxe - mn);
        if (v > 5000) v = 5000;
        const unsigned long long s0 = rt();
        while (rt() < s0 + 100 * v) __builtin_amdgcn_s_sleep(4);
    }
}

extern "C" void kernel_launch(void* const* d_in, const int* in_sizes, int n_in, void* d_out, int out_size, void* d_ws, size_t ws_size, hipStream_t stream) {
    const u16* x = (const u16*)d_in[0]; const u16* ckv = (const u16*)d_in[1]; const u16* wq = (const u16*)d_in[2]; const u16* wuk = (const u16*)d_in[3];
    const u16* wuv = (const u16*)d_in[4]; const u16* wo = (const u16*)d_in[5]; const u16* w1 = (const u16*)d_in[7]; const u16* w2 = (const u16*)d_in[8];
    float* ws = (float*)d_ws;
    float* qabs = ws; float* hid = ws + 16384; float* ml = ws + 32768; float* spart = ws + 65536; float* part = ws + 131072;
    kprobe<<<256, 64, 0, stream>>>((const char*)ckv, (unsigned long long*)((char*)d_ws + (64 << 20)));
    k1<<<256, 256, 0, stream>>>(x, wq, wuk, w1, qabs, hid);
    k2<<<256, 256, 0, stream>>>(ckv, qabs, part, ml);
    k3<<<32, 256, 0, stream>>>(part, ml, wuv, wo, spart);
    k4<<<1, 512, 0, stream>>>(x, spart, hid, w2, (u16*)d_out);
    kspin<<<1, 256, 0, stream>>>((const unsigned long long*)((char*)d_ws + (64 << 20)));
}
```

```cpp
#include <hip/hip_runtime.h>
#include <stdint.h>

#define CKV_AUX 0
#define WIN 24

namespace {
typedef unsigned short u16;
typedef unsigned v4u __attribute__((ext_vector_type(4)));
typedef unsigned v2u __attribute__((ext_vector_type(2)));
typedef float f32x2 __attribute__((ext_vector_type(2)));
typedef float f32x4 __attribute__((ext_vector_type(4)));
typedef float f32x16 __attribute__((ext_vector_type(16)));
typedef short s16x4 __attribute__((ext_vector_type(4)));
typedef __bf16 bf16x2 __attribute__((ext_vector_type(2)));
typedef __bf16 bf16x8 __attribute__((ext_vector_type(8)));
#define GAS __attribute__((address_space(1)))
#define LAS __attribute__((address_space(3)))
#define DI __device__ __forceinline__

constexpr int NWG = 256, NT = 576, FF = 1376, NFS = 172;
constexpr int AUX_SC1 = 16;
constexpr unsigned SPIN_CAP = 1u << 14;
constexpr unsigned LSPIN_CAP = 1u << 18;
constexpr float THR = 10.0f;

constexpr unsigned WS_EPOCH = 0;
constexpr unsigned WS_QX = 4096;
constexpr unsigned WS_QA = WS_QX + 131072;
constexpr unsigned WS_FLAG = WS_QA + 65536;
constexpr unsigned WS_CX = WS_FLAG + 524288;
constexpr unsigned WS_ROW = WS_CX + 1048576;
constexpr unsigned WS_FROW = WS_ROW + 1048576;
constexpr unsigned WS_PART = WS_FROW + 1048576;
constexpr unsigned WS_END = WS_PART + 8388608;
constexpr unsigned WS_STAMP = WS_END;

constexpr unsigned L_CKV = 0;
constexpr unsigned L_QS = 127 * 1024;
constexpr unsigned L_P = 128 * 1024;
constexpr unsigned L_MX = L_P + 8192;
constexpr unsigned L_AL = L_MX + 1024;
constexpr unsigned L_RED = L_AL + 512;
constexpr unsigned L_CTL = 159 * 1024;
constexpr unsigned LDS_BYTES = 160 * 1024;
enum { C_LAND = 0, C_ISSUED = 1, C_Q = 2, C_QREAD = 3, C_P = 4  , C_S1 = 8  , C_S2 = 12  , C_RED = 16, C_PUB = 17, C_N = 18 };

DI unsigned swz(unsigned r) { return ((r & 3u) << 2) | ((r >> 2) & 3u); }
DI float lo_f(unsigned u) { return __uint_as_float(u << 16); }
DI float hi_f(unsigned u) { return __uint_as_float(u & 0xffff0000u); }
DI unsigned pk_bf16(float a, float b) { f32x2 v = {a, b}; return __builtin_bit_cast(unsigned, __builtin_convertvector(v, bf16x2)); }
DI float dot2(unsigned a, unsigned b, float c) { return __builtin_amdgcn_fdot2_f32_bf16(__builtin_bit_cast(bf16x2, a), __builtin_bit_cast(bf16x2, b), c, false); }
template <int CTRL> DI float dpp(float x) { return __int_as_float(__builtin_amdgcn_update_dpp(0, __float_as_int(x), CTRL, 0xf, 0xf, true)); }
constexpr int ROR1 = 0x121, ROR2 = 0x122, ROR4 = 0x124, ROR8 = 0x128;
DI float rs32(float a, float b) { auto r = __builtin_amdgcn_permlane32_swap(__float_as_uint(a), __float_as_uint(b), false, false); return __uint_as_float(r[0]) + __uint_as_float(r[1]); }
DI float rs16(float a, float b) { auto r = __builtin_amdgcn_permlane16_swap(__float_as_uint(a), __float_as_uint(b), false, false); return __uint_as_float(r[0]) + __uint_as_float(r[1]); }
DI float mx32(float a) { auto r = __builtin_amdgcn_permlane32_swap(__float_as_uint(a), __float_as_uint(a), false, false); return fmaxf(__uint_as_float(r[0]), __uint_as_float(r[1])); }
DI float mx16(float a) { auto r = __builtin_amdgcn_permlane16_swap(__float_as_uint(a), __float_as_uint(a), false, false); return fmaxf(__uint_as_float(r[0]), __uint_as_float(r[1])); }
DI float wave_sum(float x) { x += dpp<ROR8>(x); x += dpp<ROR4>(x); x += dpp<ROR2>(x); x += dpp<ROR1>(x); x = rs16(x, x); return rs32(x, x); }
DI float rdlane(float x, int l) { return __int_as_float(__builtin_amdgcn_readlane(__float_as_int(x), l)); }
#define CFENCE() asm volatile("" ::: "memory")

DI v4u ld_sc1(__amdgpu_buffer_rsrc_t rs, unsigned off) { return __builtin_amdgcn_raw_buffer_load_b128(rs, off, 0, AUX_SC1); }
DI void st_sc1(__amdgpu_buffer_rsrc_t rs, unsigned off, v4u v) { __builtin_amdgcn_raw_buffer_store_b128(v, rs, off, 0, AUX_SC1); }
DI bool tag_ok(v4u v, unsigned tag) { return (v.y == tag) & (v.w == tag); }

DI unsigned lds_ld(LAS unsigned* p) { return __hip_atomic_load(p, __ATOMIC_RELAXED, __HIP_MEMORY_SCOPE_WORKGROUP); }
DI void lds_add(LAS unsigned* p, unsigned v, int lane) {
    CFENCE();
    if (lane == 0) (void)__hip_atomic_fetch_add(p, v, __ATOMIC_RELAXED, __HIP_MEMORY_SCOPE_WORKGROUP);
    CFENCE();
}
DI unsigned lds_wait(LAS unsigned* p, unsigned mask, unsigned want) {
    unsigned v;
    for (unsigned spins = 0;; ++spins) {
        CFENCE();
        v = __builtin_amdgcn_readfirstlane(lds_ld(p));
        if ((v & mask) >= want || spins > LSPIN_CAP) break;
    }
    CFENCE();
    return v;
}
DI void ln_stats(v4u xv, float& mean, float& rstd) {
    float e[8] = {lo_f(xv.x), hi_f(xv.x), lo_f(xv.y), hi_f(xv.y), lo_f(xv.z), hi_f(xv.z), lo_f(xv.w), hi_f(xv.w)};
    float s = ((e[0] + e[1]) + (e[2] + e[3])) + ((e[4] + e[5]) + (e[6] + e[7]));
    mean = wave_sum(s) * (1.0f / 512.0f);
    float q = 0.f;
#pragma unroll
    for (int i = 0; i < 8; ++i) { const float d = e[i] - mean; q += d * d; }
    rstd = 1.0f / sqrtf(wave_sum(q) * (1.0f / 512.0f) + 1e-5f);
}
DI float silu(float x) { return x / (1.0f + __expf(-x)); }

#define STAMP(i) do {} while (0)
#define STAMP_DECL
#define STAMP_OUT() do {} while (0)

__global__ __launch_bounds__(NT, 3) void mla_decode_kernel(const u16* __restrict__ x, const u16* __restrict__ ckv, const u16* __restrict__ wq,
                                                          const u16* __restrict__ wuk, const u16* __restrict__ wuv, const u16* __restrict__ wo,
                                                          const u16* __restrict__ w1, const u16* __restrict__ w2, u16* __restrict__ out, char* ws) {
    __shared__ __attribute__((aligned(16))) char smem[LDS_BYTES];
    LAS char* L = (LAS char*)smem;
    LAS unsigned* ctl = (LAS unsigned*)(L + L_CTL);
    const int tid = threadIdx.x, lane = tid & 63, wave = __builtin_amdgcn_readfirstlane(tid >> 6);
    const int b = blockIdx.x, xg = b & 7, jj = b >> 3, h = 4 * xg + (jj & 3), s = jj >> 2;
    const __amdgpu_buffer_rsrc_t rs = __builtin_amdgcn_make_buffer_rsrc(ws, 0, WS_END, 0x00020000);
    STAMP_DECL;
    STAMP(0);

    if (wave == 8) {
        if (lane < C_N) ctl[lane] = 0u;
        const char* src = (const char*)ckv + (size_t)b * 131072;
        unsigned xo[4];
        (void)xo;
        auto dma = [&](int row) {
            const char* g = src + row * 1024 + 16 * (lane ^ (int)swz(row & 15));
            const unsigned dst = (unsigned)(size_t)(L + L_CKV + row * 1024);
            unsigned keep;
            asm volatile("s_mov_b32 %0, m0\n\ts_mov_b32 m0, %2\n\ts_nop 0\n\tglobal_load_lds_dwordx4 %1, off\n\ts_mov_b32 m0, %0" : "=&s"(keep) : "v"(g), "s"(dst) : "memory");
        };
        asm volatile("s_waitcnt lgkmcnt(0)" ::: "memory");
        __builtin_amdgcn_s_barrier();
#pragma unroll
        for (int i = 0; i < 127; ++i) {
            dma(i);
            if (i >= WIN) {
                asm volatile("s_waitcnt vmcnt(%0)" ::"n"(WIN) : "memory");
                if (((i - WIN) & 31) == 31) { lds_add(ctl + C_LAND, 1u, lane); STAMP(1 + ((i - WIN) >> 5)); }
            }
        }
        lds_add(ctl + C_ISSUED, 1u, lane);
        if (WIN > 31) { asm volatile("s_waitcnt vmcnt(31)" ::: "memory"); lds_add(ctl + C_LAND, 1u, lane); STAMP(3); }
        lds_wait(ctl + C_QREAD, 0xffffffffu, 4u);
        dma(127);
        asm volatile("s_waitcnt vmcnt(0)" ::: "memory");
        lds_add(ctl + C_LAND, 1u, lane);
        STAMP(4);
        const int fs = 22 * xg + jj;
        if (jj < 22 && fs < NFS) {
            const unsigned ep = __hip_atomic_load((GAS unsigned*)(ws + WS_EPOCH), __ATOMIC_RELAXED, __HIP_MEMORY_SCOPE_AGENT);
            const v4u xv = *(const v4u*)(x + 8 * lane);
            v4u a[8];
#pragma unroll
            for (int i = 0; i < 8; ++i) a[i] = *(const v4u*)(w1 + (size_t)(8 * lane + i) * FF + 8 * fs);
            unsigned c[8][4];
#pragma unroll
            for (int r = 0; r < 8; ++r)
#pragma unroll
                for (int i = 0; i < 4; ++i) c[r][i] = *(const unsigned*)(w2 + (size_t)(8 * fs + r) * 512 + 2 * (lane + 64 * i));
            __builtin_amdgcn_sched_barrier(0);
            float mean, rstd;
            ln_stats(xv, mean, rstd);
            const float xe[8] = {lo_f(xv.x), hi_f(xv.x), lo_f(xv.y), hi_f(xv.y), lo_f(xv.z), hi_f(xv.z), lo_f(xv.w), hi_f(xv.w)};
            float acc[8] = {0.f, 0.f, 0.f, 0.f, 0.f, 0.f, 0.f, 0.f};
#pragma unroll
            for (int i = 0; i < 8; ++i) {
                const float xn = (xe[i] - mean) * rstd;
#pragma unroll
                for (int k = 0; k < 4; ++k) { acc[2 * k] += xn * lo_f(a[i][k]); acc[2 * k + 1] += xn * hi_f(a[i][k]); }
            }
            float r4[4], r2[2];
#pragma unroll
            for (int k = 0; k < 4; ++k) r4[k] = rs32(acc[k], acc[k + 4]);
#pragma unroll
            for (int k = 0; k < 2; ++k) { float t = rs16(r4[k], r4[k + 2]); t += dpp<ROR8>(t); t += dpp<ROR4>(t); t += dpp<ROR2>(t); t += dpp<ROR1>(t); r2[k] = silu(t); }
            float o[8] = {0.f, 0.f, 0.f, 0.f, 0.f, 0.f, 0.f, 0.f};
#pragma unroll
            for (int r = 0; r < 8; ++r) {
                const float hv = rdlane(r2[r & 1], 16 * (r >> 1));
#pragma unroll
                for (int i = 0; i < 4; ++i) { o[2 * i] += hv * lo_f(c[r][i]); o[2 * i + 1] += hv * hi_f(c[r][i]); }
            }
            const unsigned tag = (unsigned)__builtin_amdgcn_readfirstlane(ep) * 0x9E3779B1u + 0x7F4A7C15u;
#pragma unroll
            for (int i = 0; i < 4; ++i)
                st_sc1(rs, WS_FROW + (unsigned)fs * 4096u + (unsigned)(lane + 64 * i) * 16u, v4u{__float_as_uint(o[2 * i]), tag, __float_as_uint(o[2 * i + 1]), tag});
        }
        STAMP(5);
        STAMP_OUT();
        return;
    }

    const unsigned ep_raw = __hip_atomic_load((GAS unsigned*)(ws + WS_EPOCH), __ATOMIC_RELAXED, __HIP_MEMORY_SCOPE_AGENT);
    unsigned tag;

    auto gather_q = [&]() {
        unsigned pend = 0xffu;
        const unsigned hrow = (unsigned)(tid >> 7), c4 = (unsigned)(tid & 127);
        for (unsigned spins = 0; pend != 0u && spins < SPIN_CAP; ++spins) {
            CFENCE();
            v4u g[8];
#pragma unroll
            for (int i = 0; i < 8; ++i) g[i] = ld_sc1(rs, (pend >> i) & 1u ? WS_QA + (unsigned)(512 * i + tid) * 16u : 0xffffff00u);
#pragma unroll
            for (int i = 0; i < 8; ++i) {
                if (((pend >> i) & 1u) && __all(tag_ok(g[i], tag))) {
                    const unsigned head = 4u * i + hrow;
                    *(LAS v2u*)(L + L_QS + 1024u * head + 16u * ((c4 >> 1) ^ swz(head & 15u)) + 8u * (c4 & 1u)) = v2u{g[i].x, g[i].z};
                    pend &= ~(1u << i);
                }
            }
        }
        lds_add(ctl + C_Q, 1u, lane);
    };

    if (wave < 4) {
        const int w = wave;
        const v4u xv = *(const v4u*)(x + 8 * lane);
        const v2u xr = *(const v2u*)(x + 128 * w + 4 * (lane >> 1));
        v4u a[4], u[4];
#pragma unroll
        for (int i = 0; i < 4; ++i) a[i] = *(const v4u*)(wq + (size_t)(128 * w + 4 * (lane >> 1) + i) * 4096 + h * 128 + s * 16 + (lane & 1) * 8);
#pragma unroll
        for (int i = 0; i < 4; ++i) u[i] = *(const v4u*)(wuk + (size_t)(64 * s + 16 * w + 4 * (lane >> 4) + i) * 4096 + h * 128 + (lane & 15) * 8);
        __builtin_amdgcn_sched_barrier(0);
        __builtin_amdgcn_s_barrier();
        __builtin_amdgcn_sched_barrier(0);
        float mean, rstd;
        ln_stats(xv, mean, rstd);
        tag = (unsigned)__builtin_amdgcn_readfirstlane(ep_raw) * 0x9E3779B1u + 0x7F4A7C15u;
        {
            const float xe[4] = {lo_f(xr.x), hi_f(xr.x), lo_f(xr.y), hi_f(xr.y)};
            float acc[8] = {0.f, 0.f, 0.f, 0.f, 0.f, 0.f, 0.f, 0.f};
#pragma unroll
            for (int i = 0; i < 4; ++i) {
                const float xn = (xe[i] - mean) * rstd;
#pragma unroll
                for (int k = 0; k < 4; ++k) { acc[2 * k] += xn * lo_f(a[i][k]); acc[2 * k + 1] += xn * hi_f(a[i][k]); }
            }
            STAMP(1);
            float r4[4], r2[2];
#pragma unroll
            for (int k = 0; k < 4; ++k) r4[k] = rs32(acc[k], acc[k + 4]);
#pragma unroll
            for (int k = 0; k < 2; ++k) { float t = rs16(r4[k], r4[k + 2]); t += dpp<ROR8>(t); t += dpp<ROR4>(t); t += dpp<ROR2>(t); r2[k] = t; }
            if ((lane & 14) == 0)
                st_sc1(rs, WS_QX + (unsigned)(((h * 8 + s) * 4 + w) * 128 + (4 * (lane & 1) + (lane >> 4)) * 16), v4u{__float_as_uint(r2[0]), tag, __float_as_uint(r2[1]), tag});
        }
        float q8[8];
        {
            const unsigned off = WS_QX + (unsigned)(((h * 8 + ((lane & 15) >> 1)) * 4 + (lane >> 4)) * 128 + (lane & 1) * 64);
            v4u g[4];
            for (unsigned spins = 0; spins < SPIN_CAP; ++spins) {
                CFENCE();
#pragma unroll
                for (int i = 0; i < 4; ++i) g[i] = ld_sc1(rs, off + 16u * i);
                if (__all(tag_ok(g[0], tag) & tag_ok(g[1], tag) & tag_ok(g[2], tag) & tag_ok(g[3], tag))) break;
            }
            STAMP(2);
#pragma unroll
            for (int i = 0; i < 4; ++i) {
                float p0 = __uint_as_float(g[i].x), p1 = __uint_as_float(g[i].z);
                p0 = rs16(p0, p0); p1 = rs16(p1, p1);
                q8[2 * i] = rs32(p0, p0); q8[2 * i + 1] = rs32(p1, p1);
            }
        }
        {
            float r[4];
#pragma unroll
            for (int i = 0; i < 4; ++i) {
                float t0 = 0.f, t1 = 0.f;
#pragma unroll
                for (int k = 0; k < 4; ++k) { t0 += q8[2 * k] * lo_f(u[i][k]); t1 += q8[2 * k + 1] * hi_f(u[i][k]); }
                float t = t0 + t1;
                t += dpp<ROR8>(t); t += dpp<ROR4>(t); t += dpp<ROR2>(t); t += dpp<ROR1>(t);
                r[i] = t * (0.08838834764831845f * 1.4426950408889634f);
            }
            if ((lane & 15) == 0)
                st_sc1(rs, WS_QA + (unsigned)(h * 128 + 16 * s + 4 * w + (lane >> 4)) * 16u, v4u{pk_bf16(r[0], r[1]), tag, pk_bf16(r[2], r[3]), tag});
        }
        STAMP(3);
        lds_add(ctl + C_PUB, 1u, lane);
        gather_q();
        lds_wait(ctl + C_Q, 0xffffffffu, 8u);
        STAMP(4);
        const int kh = w & 1, hg = w >> 1;
        const unsigned r16 = (unsigned)(lane & 15), kg = (unsigned)(lane >> 4), fx = swz(r16);
        bf16x8 qf[16];
#pragma unroll
        for (int ks = 0; ks < 16; ++ks) qf[ks] = *(LAS bf16x8*)(L + L_QS + 1024u * (16u * hg + r16) + 256u * kg + 16u * ((unsigned)ks ^ fx));
        asm volatile("s_waitcnt lgkmcnt(0)" ::: "memory");
#pragma unroll
        for (int ks = 0; ks < 16; ++ks) asm volatile("" : "+v"(qf[ks]));
        lds_add(ctl + C_QREAD, 1u, lane);
        unsigned wv[16], wz[16];
        float m = -INFINITY;
        const int v = s >> 1, uu = s & 1;
#pragma unroll
        for (int t = 0; t < 4; ++t) {
            if (t == 3) {
                lds_wait(ctl + C_ISSUED, 0xffffffffu, 1u);
#pragma unroll
                for (int k = 0; k < 16; ++k) {
                    const int ii = 16 * w + k, sub = ii >> 3, e = ii & 7, c = 128 * v + 32 * (e & 3) + 16 * (e >> 2) + 8 * uu + sub;
                    wv[k] = *(const unsigned*)(wuv + (size_t)c * 4096 + h * 128 + 2 * lane);
                }
#pragma unroll
                for (int k = 0; k < 16; ++k) wz[k] = *(const unsigned*)(wo + (size_t)(h * 128 + 16 * s + k) * 512 + 2 * tid);
                __builtin_amdgcn_sched_barrier(0);
            }
            lds_wait(ctl + C_LAND, 0xffffffffu, (unsigned)t + 1u);
            f32x4 sc = {0.f, 0.f, 0.f, 0.f};
            {
                bf16x8 af[8];
                auto rd = [&](int ks) { return *(LAS bf16x8*)(L + L_CKV + 1024u * (32u * t + 16u * kh + r16) + 256u * kg + 16u * ((unsigned)ks ^ fx)); };
#pragma unroll
                for (int ks = 0; ks < 8; ++ks) af[ks] = rd(ks);
                __builtin_amdgcn_sched_barrier(0);
#pragma unroll
                for (int ks = 0; ks < 16; ++ks) {
                    sc = __builtin_amdgcn_mfma_f32_16x16x32_bf16(af[ks & 7], qf[ks], sc, 0, 0, 0);
                    if (ks < 8) af[ks] = rd(ks + 8);
                    __builtin_amdgcn_sched_barrier(0);
                }
            }
            LAS v2u* pdst = (LAS v2u*)(L + L_P + 2048u * t + 1024u * kh + 512u * (kg >> 1) + 16u * (16u * hg + r16) + 8u * (kg & 1u));
            const float tmax = fmaxf(fmaxf(sc[0], sc[1]), fmaxf(sc[2], sc[3]));
            bool slow = true;
            if (t == 0) {
                lds_wait(ctl + C_QREAD, 0xffffffffu, 4u);
            } else {
                const bool viol = __any(tmax - m > THR);
                *pdst = v2u{pk_bf16(__builtin_amdgcn_exp2f(sc[0] - m), __builtin_amdgcn_exp2f(sc[1] - m)), pk_bf16(__builtin_amdgcn_exp2f(sc[2] - m), __builtin_amdgcn_exp2f(sc[3] - m))};
                lds_add(ctl + C_P + t, viol ? 0x101u : 1u, lane);
                slow = (lds_wait(ctl + C_P + t, 0xffu, 4u) >> 8) != 0u;
            }
            if (slow) {
                const float wm = mx32(mx16(tmax));
                LAS float* mx = (LAS float*)(L + L_MX + 256u * t);
                if (lane < 16) mx[32 * kh + 16 * hg + lane] = wm;
                lds_add(ctl + C_S1 + t, 1u, lane);
                lds_wait(ctl + C_S1 + t, 0xffffffffu, 4u);
                m = fmaxf(m, ceilf(fmaxf(mx[16 * hg + r16], mx[32 + 16 * hg + r16])));
                *pdst = v2u{pk_bf16(__builtin_amdgcn_exp2f(sc[0] - m), __builtin_amdgcn_exp2f(sc[1] - m)), pk_bf16(__builtin_amdgcn_exp2f(sc[2] - m), __builtin_amdgcn_exp2f(sc[3] - m))};
                lds_add(ctl + C_S2 + t, 1u, lane);
            }
            STAMP(5 + t);
        }
        {
            const unsigned grp = (unsigned)(tid >> 3), sub = (unsigned)(tid & 7);
            const unsigned rec = ((8u * grp) * 4u + (unsigned)v) * 32u + (unsigned)h;
            v4u d[8];
            float mj[8], lj[8];
#pragma unroll
            for (int n = 0; n < 8; ++n) { d[n] = v4u{0u, 0u, 0u, 0u}; mj[n] = 0.f; lj[n] = 0.f; }
            unsigned pend = 0xffu;
            for (unsigned spins = 0; pend != 0u && spins < SPIN_CAP; ++spins) {
                CFENCE();
                v4u f[8];
#pragma unroll
                for (int n = 0; n < 8; ++n) f[n] = ld_sc1(rs, (pend >> n) & 1u ? WS_FLAG + (rec + 128u * n) * 16u : 0xffffff00u);
                unsigned rdy = 0u;
#pragma unroll
                for (int n = 0; n < 8; ++n) rdy |= __all(tag_ok(f[n], tag)) ? 1u << n : 0u;
                rdy &= pend;
                pend &= ~rdy;
                CFENCE();
#pragma unroll
                for (int n = 0; n < 8; ++n) {
                    if ((rdy >> n) & 1u) {
                        d[n] = ld_sc1(rs, WS_PART + (rec + 128u * n) * 256u + 128u * uu + 16u * sub);
                        mj[n] = __uint_as_float(f[n].x); lj[n] = __uint_as_float(f[n].z);
                    }
                }
            }
            STAMP(9);
            float M = fmaxf(fmaxf(fmaxf(mj[0], mj[1]), fmaxf(mj[2], mj[3])), fmaxf(fmaxf(mj[4], mj[5]), fmaxf(mj[6], mj[7])));
            M = fmaxf(M, dpp<ROR8>(M)); M = mx32(mx16(M));
            float acc[8] = {0.f, 0.f, 0.f, 0.f, 0.f, 0.f, 0.f, 0.f}, Ls = 0.f;
#pragma unroll
            for (int n = 0; n < 8; ++n) {
                const float wt = __builtin_amdgcn_exp2f(mj[n] - M);
                Ls += wt * lj[n];
                const unsigned wh = __float_as_uint(wt) & 0xffff0000u, wl = wh >> 16;
#pragma unroll
                for (int k = 0; k < 4; ++k) { acc[2 * k] = dot2(d[n][k], wl, acc[2 * k]); acc[2 * k + 1] = dot2(d[n][k], wh, acc[2 * k + 1]); }
            }
            float r4[4], r2[2];
#pragma unroll
            for (int k = 0; k < 4; ++k) r4[k] = rs32(acc[k], acc[k + 4]);
#pragma unroll
            for (int k = 0; k < 2; ++k) { float t = rs16(r4[k], r4[k + 2]); t += dpp<ROR8>(t); r2[k] = t; }
            Ls += dpp<ROR8>(Ls); Ls = rs16(Ls, Ls); Ls = rs32(Ls, Ls);
            LAS float* red = (LAS float*)(L + L_RED);
            if ((lane & 8) == 0) *(LAS f32x2*)(red + 64 * w + 8 * sub + 2 * (lane >> 4)) = f32x2{r2[0], r2[1]};
            if (lane == 0) *(LAS f32x2*)(red + 256 + 2 * w) = f32x2{M, Ls};
            lds_add(ctl + C_RED, 1u, lane);
            lds_wait(ctl + C_RED, 0xffffffffu, 4u);
            STAMP(10);
            const f32x4 ml0 = *(LAS f32x4*)(red + 256), ml1 = *(LAS f32x4*)(red + 260);
            const float Mg = fmaxf(fmaxf(ml0[0], ml0[2]), fmaxf(ml1[0], ml1[2]));
            const float e0 = __builtin_amdgcn_exp2f(ml0[0] - Mg), e1 = __builtin_amdgcn_exp2f(ml0[2] - Mg), e2 = __builtin_amdgcn_exp2f(ml1[0] - Mg), e3 = __builtin_amdgcn_exp2f(ml1[2] - Mg);
            const float Lg = (e0 * ml0[1] + e1 * ml0[3]) + (e2 * ml1[1] + e3 * ml1[3]);
            const int ii = 16 * w + (lane & 15);
            const float cv = ((e0 * red[ii] + e1 * red[64 + ii]) + (e2 * red[128 + ii] + e3 * red[192 + ii])) / Lg;
            float c0 = 0.f, c1 = 0.f;
#pragma unroll
            for (int k = 0; k < 16; ++k) { const float t = rdlane(cv, k); c0 += t * lo_f(wv[k]); c1 += t * hi_f(wv[k]); }
            st_sc1(rs, WS_CX + (unsigned)(((h * 8 + s) * 4 + w) * 1024 + lane * 16), v4u{__float_as_uint(c0), tag, __float_as_uint(c1), tag});
        }
        STAMP(11);
        {
            const unsigned off = WS_CX + (unsigned)((h * 32 + (lane >> 3)) * 1024 + (8 * s + (lane & 7)) * 16);
            v4u g[4];
            for (unsigned spins = 0; spins < SPIN_CAP; ++spins) {
                CFENCE();
#pragma unroll
                for (int i = 0; i < 4; ++i) g[i] = ld_sc1(rs, off + 8192u * i);
                if (__all(tag_ok(g[0], tag) & tag_ok(g[1], tag) & tag_ok(g[2], tag) & tag_ok(g[3], tag))) break;
            }
            STAMP(12);
            float p0 = (__uint_as_float(g[0].x) + __uint_as_float(g[1].x)) + (__uint_as_float(g[2].x) + __uint_as_float(g[3].x));
            float p1 = (__uint_as_float(g[0].z) + __uint_as_float(g[1].z)) + (__uint_as_float(g[2].z) + __uint_as_float(g[3].z));
            p0 += dpp<ROR8>(p0); p1 += dpp<ROR8>(p1);
            float pp = rs16(p0, p1);
            pp = rs32(pp, pp);
            float o0 = 0.f, o1 = 0.f;
#pragma unroll
            for (int k = 0; k < 16; ++k) { const float t = rdlane(pp, (k >> 1) + 16 * (k & 1)); o0 += t * lo_f(wz[k]); o1 += t * hi_f(wz[k]); }
            st_sc1(rs, WS_ROW + (unsigned)(b * 4096 + tid * 16), v4u{__float_as_uint(o0), tag, __float_as_uint(o1), tag});
        }
        STAMP(13);
        if (w == 0) {
            const unsigned cp = (unsigned)(32 * xg + jj);
            const unsigned xo = *(const unsigned*)(x + 2 * cp);
            v4u g[4], f[3];
            for (unsigned spins = 0; spins < SPIN_CAP; ++spins) {
                CFENCE();
#pragma unroll
                for (int i = 0; i < 3; ++i) f[i] = ld_sc1(rs, WS_FROW + (unsigned)min(lane + 64 * i, NFS - 1) * 4096u + cp * 16u);
                if (__all(tag_ok(f[0], tag) & tag_ok(f[1], tag) & tag_ok(f[2], tag))) break;
            }
            const bool f2ok = lane + 128 < NFS;
            const float g0 = wave_sum(__uint_as_float(f[0].x) + __uint_as_float(f[1].x) + (f2ok ? __uint_as_float(f[2].x) : 0.f));
            const float g1 = wave_sum(__uint_as_float(f[0].z) + __uint_as_float(f[1].z) + (f2ok ? __uint_as_float(f[2].z) : 0.f));
            const float gate0 = silu(g0), gate1 = silu(g1);
            for (unsigned spins = 0; spins < SPIN_CAP; ++spins) {
                CFENCE();
#pragma unroll
                for (int i = 0; i < 4; ++i) g[i] = ld_sc1(rs, WS_ROW + (unsigned)(lane + 64 * i) * 4096u + cp * 16u);
                if (__all(tag_ok(g[0], tag) & tag_ok(g[1], tag) & tag_ok(g[2], tag) & tag_ok(g[3], tag))) break;
            }
            STAMP(14);
            const float s0 = wave_sum((__uint_as_float(g[0].x) + __uint_as_float(g[1].x)) + (__uint_as_float(g[2].x) + __uint_as_float(g[3].x)));
            const float s1 = wave_sum((__uint_as_float(g[0].z) + __uint_as_float(g[1].z)) + (__uint_as_float(g[2].z) + __uint_as_float(g[3].z)));
            if (lane == 0) {
                *(unsigned*)(out + 2 * cp) = pk_bf16(lo_f(xo) + s0 * gate0, hi_f(xo) + s1 * gate1);
                if (b == 0) __hip_atomic_store((GAS unsigned*)(ws + WS_EPOCH), ep_raw + 1u, __ATOMIC_RELAXED, __HIP_MEMORY_SCOPE_AGENT);
            }
        }
        STAMP(15);
        STAMP_OUT();
    } else {
        const int v = wave - 4;
        __builtin_amdgcn_sched_barrier(0);
        __builtin_amdgcn_s_barrier();
        __builtin_amdgcn_sched_barrier(0);
        lds_wait(ctl + C_PUB, 0xffffffffu, 1u);
        tag = (unsigned)__builtin_amdgcn_readfirstlane(ep_raw) * 0x9E3779B1u + 0x7F4A7C15u;
        gather_q();
        STAMP(4);
        f32x16 o[4];
#pragma unroll
        for (int ct = 0; ct < 4; ++ct)
#pragma unroll
            for (int i = 0; i < 16; ++i) o[ct][i] = 0.f;
        float m = -INFINITY, l = 0.f;
        const unsigned hh = (unsigned)(lane >> 5), blk = (unsigned)((lane >> 4) & 1), qq = (unsigned)((lane & 15) >> 2), pp = (unsigned)(lane & 3);
#pragma unroll
        for (int t = 0; t < 4; ++t) {
            lds_wait(ctl + C_LAND, 0xffffffffu, (unsigned)t + 1u);
            s16x4 bt[2][4][2];
#pragma unroll
            for (int ks = 0; ks < 2; ++ks)
#pragma unroll
                for (int ct = 0; ct < 4; ++ct)
#pragma unroll
                    for (int tt = 0; tt < 2; ++tt) {
                        const unsigned row = 16u * ks + 8u * hh + 4u * tt + qq;
                        const unsigned ch = (4u * ct + 2u * blk + (pp >> 1)) ^ ((qq << 2) | (2u * hh + tt));
                        bt[ks][ct][tt] = __builtin_amdgcn_ds_read_tr16_b64_v4i16((LAS s16x4*)(L + L_CKV + 1024u * (32u * t + row) + 256u * v + 16u * ch + 8u * (pp & 1u)));
                    }
            bool slow = true;
            if (t > 0) slow = (lds_wait(ctl + C_P + t, 0xffu, 4u) >> 8) != 0u;
            if (slow) {
                lds_wait(ctl + C_S1 + t, 0xffffffffu, 4u);
                LAS float* mx = (LAS float*)(L + L_MX + 256u * t);
                const float mn = fmaxf(m, ceilf(fmaxf(mx[lane & 31], mx[32 + (lane & 31)])));
                const float al = __builtin_amdgcn_exp2f(m - mn);
                m = mn;
                if (t > 0) {
                    l *= al;
                    LAS float* ab = (LAS float*)(L + L_AL + 128u * v);
                    if (lane < 32) ab[lane] = al;
                    CFENCE();
#pragma unroll
                    for (int q4 = 0; q4 < 4; ++q4) {
                        const f32x4 a4 = *(LAS f32x4*)(ab + 8 * q4 + 4 * hh);
#pragma unroll
                        for (int ct = 0; ct < 4; ++ct)
#pragma unroll
                            for (int i = 0; i < 4; ++i) o[ct][4 * q4 + i] *= a4[i];
                    }
                }
                lds_wait(ctl + C_S2 + t, 0xffffffffu, 4u);
            }
            const v4u p0 = *(LAS v4u*)(L + L_P + 2048u * t + 16u * lane), p1 = *(LAS v4u*)(L + L_P + 2048u * t + 1024u + 16u * lane);
#pragma unroll
            for (int ct = 0; ct < 4; ++ct) {
                o[ct] = __builtin_amdgcn_mfma_f32_32x32x16_bf16(__builtin_bit_cast(bf16x8, p0), __builtin_bit_cast(bf16x8, __builtin_shufflevector(bt[0][ct][0], bt[0][ct][1], 0, 1, 2, 3, 4, 5, 6, 7)), o[ct], 0, 0, 0);
                o[ct] = __builtin_amdgcn_mfma_f32_32x32x16_bf16(__builtin_bit_cast(bf16x8, p1), __builtin_bit_cast(bf16x8, __builtin_shufflevector(bt[1][ct][0], bt[1][ct][1], 0, 1, 2, 3, 4, 5, 6, 7)), o[ct], 0, 0, 0);
            }
#pragma unroll
            for (int k = 0; k < 4; ++k) { l = dot2(p0[k], 0x3f803f80u, l); l = dot2(p1[k], 0x3f803f80u, l); }
            STAMP(5 + t);
        }
        const unsigned pbase = WS_PART + (unsigned)(b * 4 + v) * 8192u + 16u * (unsigned)(lane & 15);
#pragma unroll
        for (int i = 0; i < 8; ++i) {
            const int ra = 2 * i, rb = 2 * i + 1;
            const unsigned a0 = pk_bf16(o[0][ra], o[1][ra]), a1 = pk_bf16(o[2][ra], o[3][ra]);
            const unsigned b0 = pk_bf16(o[0][rb], o[1][rb]), b1 = pk_bf16(o[2][rb], o[3][rb]);
            const auto s0 = __builtin_amdgcn_permlane16_swap(a0, b0, false, false);
            const auto s1 = __builtin_amdgcn_permlane16_swap(a1, b1, false, false);
            const unsigned head = (unsigned)((ra & 3) + 8 * (ra >> 2)) + 4u * hh + blk;
            st_sc1(rs, pbase + 256u * head, v4u{s0[0], s1[0], s0[1], s1[1]});
        }
        STAMP(9);
        const float lt = rs32(l, l);
        asm volatile("s_waitcnt vmcnt(0)" ::: "memory");
        STAMP(10);
        if (lane < 32) st_sc1(rs, WS_FLAG + (unsigned)((b * 4 + v) * 32 + lane) * 16u, v4u{__float_as_uint(m), tag, __float_as_uint(lt), tag});
        STAMP_OUT();
    }
}
}

extern "C" void kernel_launch(void* const* d_in, const int* in_sizes, int n_in, void* d_out, int out_size, void* d_ws, size_t ws_size, hipStream_t stream) {
    (void)in_sizes; (void)n_in; (void)out_size; (void)ws_size;
    mla_decode_kernel<<<NWG, NT, 0, stream>>>((const u16*)d_in[0], (const u16*)d_in[1], (const u16*)d_in[2], (const u16*)d_in[3], (const u16*)d_in[4],
                                              (const u16*)d_in[5], (const u16*)d_in[7], (const u16*)d_in[8], (u16*)d_out, (char*)d_ws);
}
```

```cpp
#include <hip/hip_runtime.h>
#include <stdint.h>
#pragma clang diagnostic ignored "-Wint-to-pointer-cast"

#define WIN 44
#define D_LE 24
#define D_U 0
#define D_A 14
#define D_G 16
#define D_T 0
#define D_F 4
#define D_H 16
#define D_E 7
#define D_P 2
#define L_GO_WORD C_QP
#define L_GO 8
#define D_E2 2
#define T_AT 2
#define D_1 8
#define D_2 14
#define D_3 14
#define QK_PRE 3
#define PV_PRE 8
#define CODE_LINES 168

namespace {
typedef unsigned short u16;
typedef unsigned v4u __attribute__((ext_vector_type(4)));
typedef unsigned v2u __attribute__((ext_vector_type(2)));
typedef float f32x2 __attribute__((ext_vector_type(2)));
typedef float f32x4 __attribute__((ext_vector_type(4)));
typedef float f32x16 __attribute__((ext_vector_type(16)));
typedef short s16x4 __attribute__((ext_vector_type(4)));
typedef __bf16 bf16x2 __attribute__((ext_vector_type(2)));
typedef __bf16 bf16x8 __attribute__((ext_vector_type(8)));
typedef __amdgpu_buffer_rsrc_t rsrc_t;
#define GAS __attribute__((address_space(1)))
#define LAS __attribute__((address_space(3)))
#define DI __device__ __forceinline__

constexpr int NWG = 256, NT = 576, FF = 1376, NFS = 172;
constexpr int AUX_SC1 = 16, AUX_NT = 2;
constexpr unsigned SPIN_CAP = 1u << 14;
constexpr unsigned LSPIN_CAP = 1u << 18;

constexpr unsigned WS_EPOCH = 0;
constexpr unsigned WS_QX = 4096;
constexpr unsigned WS_QA = WS_QX + 262144;
constexpr unsigned WS_FLAG = WS_QA + 65536;
constexpr unsigned WS_ML = WS_FLAG + 65536;
constexpr unsigned WS_CX = WS_ML + 131072;
constexpr unsigned WS_ROW = WS_CX + 262144;
constexpr unsigned WS_HX = WS_ROW + 262144;
constexpr unsigned WS_PART = WS_HX + 262144;
constexpr unsigned WS_END = WS_PART + 8388608;
constexpr unsigned WS_STAMP = WS_END;
constexpr unsigned OOB = 0xffffff00u;

constexpr unsigned L_CKV = 0;
constexpr unsigned L_QS = 128 * 1024;
constexpr unsigned L_QP = L_QS;
constexpr unsigned L_QF = L_QP + 2048;
constexpr unsigned L_CTL = L_QF + 512;
constexpr unsigned L_JUNK = L_CTL + 256;
constexpr unsigned L_P = L_QS;
constexpr unsigned L_MX = L_P + 8192;
constexpr unsigned L_LS = L_MX + 512;
constexpr unsigned L_RED = L_LS + 1024;
constexpr unsigned L_RED2 = L_RED + 1024;
constexpr unsigned L_RED3 = L_RED2 + 2048;
constexpr unsigned L_MLW = L_RED3 + 1024;
constexpr unsigned L_FH = L_MLW + 64;
constexpr unsigned LDS_BYTES = 160 * 1024;
enum { C_QP = 0, C_QF = 1, C_N = 2 };

DI constexpr unsigned swz(unsigned r) { return ((r & 3u) << 2) | ((r >> 2) & 3u); }
DI float lo_f(unsigned u) { return __uint_as_float(u << 16); }
DI float hi_f(unsigned u) { return __uint_as_float(u & 0xffff0000u); }
DI unsigned pk_bf16(float a, float b) { f32x2 v = {a, b}; return __builtin_bit_cast(unsigned, __builtin_convertvector(v, bf16x2)); }
DI float dot2(unsigned a, unsigned b, float c) { return __builtin_amdgcn_fdot2_f32_bf16(__builtin_bit_cast(bf16x2, a), __builtin_bit_cast(bf16x2, b), c, false); }
template <int CTRL> DI float dpp(float x) { return __int_as_float(__builtin_amdgcn_update_dpp(0, __float_as_int(x), CTRL, 0xf, 0xf, true)); }
constexpr int QX1 = 0xB1, QX2 = 0x4E, HMIR = 0x141;
constexpr int ROR1 = 0x121, ROR2 = 0x122, ROR4 = 0x124, ROR8 = 0x128;
DI float rs32(float a, float b) { auto r = __builtin_amdgcn_permlane32_swap(__float_as_uint(a), __float_as_uint(b), false, false); return __uint_as_float(r[0]) + __uint_as_float(r[1]); }
DI float rs16(float a, float b) { auto r = __builtin_amdgcn_permlane16_swap(__float_as_uint(a), __float_as_uint(b), false, false); return __uint_as_float(r[0]) + __uint_as_float(r[1]); }
DI float mx32(float a) { auto r = __builtin_amdgcn_permlane32_swap(__float_as_uint(a), __float_as_uint(a), false, false); return fmaxf(__uint_as_float(r[0]), __uint_as_float(r[1])); }
DI float mx16(float a) { auto r = __builtin_amdgcn_permlane16_swap(__float_as_uint(a), __float_as_uint(a), false, false); return fmaxf(__uint_as_float(r[0]), __uint_as_float(r[1])); }
DI float max3(float a, float b, float c) { float r; asm("v_max3_f32 %0, %1, %2, %3" : "=v"(r) : "v"(a), "v"(b), "v"(c)); return r; }
DI float wave_sum(float x) { x += dpp<ROR8>(x); x += dpp<ROR4>(x); x += dpp<ROR2>(x); x += dpp<ROR1>(x); x = rs16(x, x); return rs32(x, x); }
DI float rdlane(float x, int l) { return __int_as_float(__builtin_amdgcn_readlane(__float_as_int(x), l)); }
#define CFENCE() asm volatile("" ::: "memory")
#define PIN() __builtin_amdgcn_sched_barrier(0)

DI rsrc_t mk_rsrc(const void* p, unsigned bytes) { return __builtin_amdgcn_make_buffer_rsrc((void*)p, 0, bytes, 0x00020000); }
DI v4u ld_sc1(rsrc_t rs, unsigned off) { return __builtin_amdgcn_raw_buffer_load_b128(rs, off, 0, AUX_SC1); }
DI void st_sc1(rsrc_t rs, unsigned off, v4u v) { __builtin_amdgcn_raw_buffer_store_b128(v, rs, off, 0, AUX_SC1); }
DI bool tag_ok(v4u v, unsigned tag) { return (v.y == tag) & (v.w == tag); }
DI unsigned mk_tag(unsigned ep) { return ep * 0x9E3779B1u + 0x7F4A7C15u; }
DI unsigned ld_epoch(const char* ws) { unsigned e; asm volatile("s_load_dword %0, %1, 0x0\n\ts_waitcnt lgkmcnt(0)" : "=&s"(e) : "s"(ws) : "memory"); return e; }

DI void lds_add(unsigned addr, unsigned v) {
    const unsigned a = addr, x = v;
    asm volatile("s_mov_b64 exec, 1\n\tds_add_u32 %0, %1\n\ts_mov_b64 exec, -1" ::"v"(a), "v"(x) : "memory");
}
DI unsigned lds_ld(unsigned addr) { return __builtin_amdgcn_readfirstlane(__hip_atomic_load((LAS unsigned*)addr, __ATOMIC_RELAXED, __HIP_MEMORY_SCOPE_WORKGROUP)); }
DI void lds_wait(unsigned addr, unsigned want) {
    unsigned t, n = LSPIN_CAP;
    asm volatile("1:\n\tds_read_b32 %0, %2\n\ts_sub_u32 %1, %1, 1\n\ts_cbranch_scc1 2f\n\ts_waitcnt lgkmcnt(0)\n\tv_cmp_le_u32 vcc, %3, %0\n\ts_cbranch_vccz 1b\n2:\n\ts_waitcnt lgkmcnt(0)"
                 : "=&v"(t), "+s"(n) : "v"(addr), "s"(want) : "vcc", "scc", "memory");
}
#define BAR() do { asm volatile("s_waitcnt lgkmcnt(0)\n\ts_barrier" ::: "memory"); } while (0)
#define CTL(i) (L_CTL + 4u * (unsigned)(i))
DI void ln_stats(v4u xv, float& mean, float& rstd) {
    float e[8] = {lo_f(xv.x), hi_f(xv.x), lo_f(xv.y), hi_f(xv.y), lo_f(xv.z), hi_f(xv.z), lo_f(xv.w), hi_f(xv.w)};
    float s = ((e[0] + e[1]) + (e[2] + e[3])) + ((e[4] + e[5]) + (e[6] + e[7]));
    mean = wave_sum(s) * (1.0f / 512.0f);
    float q = 0.f;
#pragma unroll
    for (int i = 0; i < 8; ++i) { const float d = e[i] - mean; q += d * d; }
    rstd = __builtin_amdgcn_rsqf(wave_sum(q) * (1.0f / 512.0f) + 1e-5f);
}
DI f32x2 gemv4(const v4u (&wt)[4], f32x4 c) {
    float acc[8];
#pragma unroll
    for (int k = 0; k < 4; ++k) { acc[2 * k] = c[0] * lo_f(wt[0][k]); acc[2 * k + 1] = c[0] * hi_f(wt[0][k]); }
#pragma unroll
    for (int i = 1; i < 4; ++i)
#pragma unroll
        for (int k = 0; k < 4; ++k) { acc[2 * k] += c[i] * lo_f(wt[i][k]); acc[2 * k + 1] += c[i] * hi_f(wt[i][k]); }
    const float r0 = rs32(acc[0], acc[4]), r1 = rs32(acc[1], acc[5]), r2 = rs32(acc[2], acc[6]), r3 = rs32(acc[3], acc[7]);
    return f32x2{rs16(r0, r2), rs16(r1, r3)};
}
DI f32x2 gemv4(const float (&wt)[4][8], f32x4 c) {
    float acc[8];
#pragma unroll
    for (int k = 0; k < 8; ++k) acc[k] = c[0] * wt[0][k];
#pragma unroll
    for (int i = 1; i < 4; ++i)
#pragma unroll
        for (int k = 0; k < 8; ++k) acc[k] += c[i] * wt[i][k];
    const float r0 = rs32(acc[0], acc[4]), r1 = rs32(acc[1], acc[5]), r2 = rs32(acc[2], acc[6]), r3 = rs32(acc[3], acc[7]);
    return f32x2{rs16(r0, r2), rs16(r1, r3)};
}
DI float silu(float x) { return x * __builtin_amdgcn_rcpf(1.0f + __builtin_amdgcn_exp2f(-1.4426950408889634f * x)); }

#define STAMP(i) do {} while (0)
#define STAMPT(i, t) do {} while (0)
#define STAMP_DECL
#define STAMP_OUT() do {} while (0)

template <int W> DI void dma4(unsigned vx, const char* gbase, unsigned ldst) {
    unsigned t1, t2, t3, keep;
    asm volatile(
        "s_mov_b32 %3, m0\n\ts_mov_b32 m0, %6\n\t"
        "v_xor_b32 %0, 64, %4\n\tv_xor_b32 %1, 0x80, %4\n\tv_xor_b32 %2, 0xc0, %4\n\t"
        "s_waitcnt vmcnt(%c7)\n\tglobal_load_lds_dwordx4 %4, %5 nt\n\t"
        "s_waitcnt vmcnt(%c7)\n\tglobal_load_lds_dwordx4 %0, %5 offset:1024 nt\n\t"
        "s_waitcnt vmcnt(%c7)\n\tglobal_load_lds_dwordx4 %1, %5 offset:2048 nt\n\t"
        "s_waitcnt vmcnt(%c7)\n\tglobal_load_lds_dwordx4 %2, %5 offset:3072 nt\n\t"
        "s_mov_b32 m0, %3"
        : "=&v"(t1), "=&v"(t2), "=&v"(t3), "=&s"(keep)
        : "v"(vx), "s"(gbase), "s"(ldst), "i"(W - 1)
        : "memory");
}
DI void touch(unsigned voff, const void* gbase) {
    unsigned keep;
    asm volatile("s_mov_b32 %0, m0\n\ts_mov_b32 m0, %3\n\ts_nop 0\n\tglobal_load_lds_dword %1, %2\n\ts_mov_b32 m0, %0"
                 : "=&s"(keep) : "v"(voff), "s"(gbase), "s"(L_JUNK) : "memory");
}
DI void lds_wait_idle(unsigned addr, unsigned want) {
    unsigned t, n = LSPIN_CAP;
    asm volatile("1:\n\ts_sleep 1\n\tds_read_b32 %0, %2\n\ts_sub_u32 %1, %1, 1\n\ts_cbranch_scc1 2f\n\ts_waitcnt lgkmcnt(0)\n\tv_cmp_le_u32 vcc, %3, %0\n\ts_cbranch_vccz 1b\n2:\n\ts_waitcnt lgkmcnt(0)"
                 : "=&v"(t), "+s"(n) : "v"(addr), "s"(want) : "vcc", "scc", "memory");
}

__global__ __launch_bounds__(NT, 3) void mla_decode_kernel(const u16* __restrict__ x, const u16* __restrict__ ckv, const u16* __restrict__ wq,
                                                          const u16* __restrict__ wuk, const u16* __restrict__ wuv, const u16* __restrict__ wo,
                                                          const u16* __restrict__ w1, const u16* __restrict__ w2, u16* __restrict__ out, char* ws) {
    __shared__ __attribute__((aligned(16))) char smem[LDS_BYTES];
    LAS char* L = (LAS char*)smem;
    const int tid = threadIdx.x, lane = tid & 63, wave = __builtin_amdgcn_readfirstlane(tid >> 6);
    const int b = blockIdx.x, xg = b & 7, h = b & 31, s = b >> 5, hs = 8 * h + s;
    const bool late = s < 4;
    const int w = wave & 3;
    STAMP_DECL;
    STAMP(0);
    if (wave == 7) {
        unsigned long long pc;
        asm volatile("s_getpc_b64 %0" : "=s"(pc));
        touch((unsigned)min((s & 3) * 64 + lane, CODE_LINES - 1) * 128u, (const void*)(pc & ~255ull));
    }
    const unsigned goff = WS_QA + (unsigned)tid * 16u;
    auto gather_issue = [&](const rsrc_t rs, v4u (&g)[8]) {
#pragma unroll
        for (int i = 0; i < 8; ++i) g[i] = ld_sc1(rs, goff + 8192u * i);
        PIN();
    };
    auto gather_stage = [&](const unsigned tag, v4u (&g)[8]) {
        const unsigned hrow = (unsigned)(tid >> 7), c4 = (unsigned)(tid & 127);
        bool ok = true;
#pragma unroll
        for (int i = 0; i < 8; ++i) {
            ok &= tag_ok(g[i], tag);
            const unsigned fz = (hrow << 2) | (unsigned)(i & 3);
            *(LAS v2u*)(L_QS + 4096u * i + 1024u * hrow + 16u * ((c4 >> 1) ^ fz) + 8u * (c4 & 1u)) = v2u{g[i].x, g[i].z};
            PIN();
        }
        return (bool)__all(ok);
    };
    auto gather_finish = [&](const rsrc_t rs, const unsigned tag, v4u (&g)[8]) {
        if (!gather_stage(tag, g)) {
#pragma unroll 1
            for (unsigned spins = 0; spins < SPIN_CAP; ++spins) {
                CFENCE();
                v4u g2[8];
                gather_issue(rs, g2);
                if (gather_stage(tag, g2)) break;
            }
        }
    };
    if (wave >= 4 && wave < 8) {
        if (tid - 256 < C_N) *(LAS unsigned*)(L + L_CTL + 4 * (tid - 256)) = 0u;
        BAR();
        const unsigned tag = mk_tag(ld_epoch(ws));
        const rsrc_t rs = mk_rsrc(ws, WS_END);
        const int job = 8 * (xg + 8 * ((b - 128) >> 5)) + 2 * ((b >> 3) & 3) + w;
        const bool ffn = !late && w < 2 && job < 176;
        if (w == 3) {
            {
                const unsigned k = (unsigned)lane & 31u;
                unsigned o = WS_FLAG + (unsigned)(xg * 8192 + (b >> 3) * 256) + 128u * (k & 1u);
                if (k >= 4u) o = WS_ML + (unsigned)b * 512u + 128u * (k & 3u);
                if (k >= 8u) o = WS_CX + (unsigned)hs * 1024u + 128u * (k & 7u);
                if (k >= 16u) o = WS_ROW + (unsigned)hs * 512u + 128u * (k & 3u);
                touch(o, ws);
            }
            asm volatile("s_waitcnt vmcnt(0)" ::: "memory");
        } else if (w == 0 && late) {
            lds_wait_idle(CTL(C_QP), 4u);
            const f32x2 p0 = *(LAS f32x2*)(L + L_QP + 8 * lane), p1 = *(LAS f32x2*)(L + L_QP + 512 + 8 * lane);
            const f32x2 p2 = *(LAS f32x2*)(L + L_QP + 1024 + 8 * lane), p3 = *(LAS f32x2*)(L + L_QP + 1536 + 8 * lane);
            const f32x2 p = (p0 + p1) + (p2 + p3);
            st_sc1(rs, WS_QX + (unsigned)((4 * h + s) * 1024 + lane * 16), v4u{__float_as_uint(p[0]), tag, __float_as_uint(p[1]), tag});
            STAMP(1);
            __builtin_amdgcn_s_sleep(D_A);
#pragma unroll 1
            for (unsigned spins = 0; spins < SPIN_CAP; ++spins) {
                CFENCE();
                v4u g[4];
#pragma unroll
                for (int i = 0; i < 4; ++i) g[i] = ld_sc1(rs, i != s ? WS_QX + (unsigned)((h * 4 + i) * 1024 + lane * 16) : OOB);
                PIN();
                bool ok = true;
                float qs[4][2];
#pragma unroll
                for (int i = 0; i < 4; ++i) {
                    ok &= tag_ok(g[i], tag) | (i == s);
                    qs[i][0] = i == s ? p[0] : __uint_as_float(g[i].x); qs[i][1] = i == s ? p[1] : __uint_as_float(g[i].z);
                }
                if (__all(ok)) {
                    *(LAS f32x2*)(L + L_QF + 8 * lane) = f32x2{(qs[0][0] + qs[1][0]) + (qs[2][0] + qs[3][0]), (qs[0][1] + qs[1][1]) + (qs[2][1] + qs[3][1])};
                    lds_add(CTL(C_QF), 1u);
                    break;
                }
            }
            STAMP(2);
        } else if (ffn) {
            const int fg = job >> 4, fsl = job & 15;
            const rsrc_t r1 = mk_rsrc(w1, 512 * FF * 2);
            const unsigned fcol = 256u * fg + 16u * (lane & 15);
            const unsigned foff = fcol < FF * 2 ? (unsigned)(32 * fsl + 8 * (lane >> 4)) * (FF * 2) + fcol : 0x7fffff00u;
            __builtin_amdgcn_s_sleep(D_F);
            const v4u xv = *(const v4u*)(x + 8 * lane), xr = *(const v4u*)(x + 32 * fsl + 8 * (lane >> 4));
            v4u fa[2][4];
#pragma unroll
            for (int i = 0; i < 8; ++i) fa[i >> 2][i & 3] = __builtin_amdgcn_raw_buffer_load_b128(r1, foff, i * FF * 2, AUX_NT);
            PIN();
            float mean, rstd;
            ln_stats(xv, mean, rstd);
            const f32x4 x0 = {(lo_f(xr.x) - mean) * rstd, (hi_f(xr.x) - mean) * rstd, (lo_f(xr.y) - mean) * rstd, (hi_f(xr.y) - mean) * rstd};
            const f32x4 x1 = {(lo_f(xr.z) - mean) * rstd, (hi_f(xr.z) - mean) * rstd, (lo_f(xr.w) - mean) * rstd, (hi_f(xr.w) - mean) * rstd};
            const f32x2 hp = gemv4(fa[0], x0) + gemv4(fa[1], x1);
            st_sc1(rs, WS_HX + (unsigned)job * 1024u + 64u * (lane & 15) + 16u * (lane >> 4), v4u{__float_as_uint(hp[0]), tag, __float_as_uint(hp[1]), tag});
            STAMP(1);
        }
        BAR();
        {
            v4u g[8];
            gather_issue(rs, g);
            asm volatile("s_barrier" ::: "memory");
            PIN();
            gather_finish(rs, tag, g);
        }
        STAMP(4);
        const int v = w;
        const unsigned hh = (unsigned)(lane >> 5), blk = (unsigned)((lane >> 4) & 1), qq = (unsigned)((lane & 15) >> 2), pp = (unsigned)(lane & 3);
        unsigned b0[2][8];
#pragma unroll
        for (int j = 0; j < 8; ++j) {
            b0[0][j] = (L_CKV + 1024u * (4u * hh + qq) + 256u * v + 16u * ((2u * blk + (pp >> 1)) ^ ((qq << 2) | hh)) + 8u * (pp & 1u)) ^ (64u * (j >> 1) + 32u * (j & 1));
            b0[1][j] = b0[0][j] + 65536u;
            asm volatile("" : "+v"(b0[0][j]), "+v"(b0[1][j]));
        }
        unsigned pbase = WS_PART + (unsigned)b * 32768u + 256u * v + 16u * (unsigned)(lane & 15) + 1024u * (4u * hh + blk);
        unsigned pb[4] = {pbase, pbase + 8192u, pbase + 16384u, pbase + 24576u};
        asm volatile("" : "+v"(pb[0]), "+v"(pb[1]), "+v"(pb[2]), "+v"(pb[3]));
        auto rd_b = [&](int n) {
            const int kk = n >> 2, ct = n & 3;
            const unsigned o = 16384u * (kk & 3);
            const s16x4 lo = __builtin_amdgcn_ds_read_tr16_b64_v4i16((LAS s16x4*)(b0[kk >> 2][2 * ct] + o));
            const s16x4 hi = __builtin_amdgcn_ds_read_tr16_b64_v4i16((LAS s16x4*)(b0[kk >> 2][2 * ct + 1] + o + 8192u));
            return __builtin_bit_cast(bf16x8, __builtin_shufflevector(lo, hi, 0, 1, 2, 3, 4, 5, 6, 7));
        };
        BAR();
        BAR();
        BAR();
        __builtin_amdgcn_s_sleep(1);
        bf16x8 bf[32];
#pragma unroll
        for (int n = 0; n < PV_PRE; ++n) bf[n] = rd_b(n);
        PIN();
        asm volatile("s_barrier" ::: "memory");
        PIN();
        STAMP(7);
        bf16x8 pf[8];
#pragma unroll
        for (int kk = 0; kk < 8; ++kk) pf[kk] = *(LAS bf16x8*)(L_P + 16u * lane + 1024u * kk);
        PIN();
        f32x16 o[4];
#pragma unroll
        for (int ct = 0; ct < 4; ++ct)
#pragma unroll
            for (int i = 0; i < 16; ++i) o[ct][i] = 0.f;
#pragma unroll
        for (int n = 0; n < 32; ++n) {
            o[n & 3] = __builtin_amdgcn_mfma_f32_32x32x16_bf16(pf[n >> 2], bf[n], o[n & 3], 0, 0, 0);
            PIN();
            if (n + PV_PRE < 32) { bf[n + PV_PRE] = rd_b(n + PV_PRE); PIN(); }
        }
        STAMP(8);
#pragma unroll
        for (int i = 0; i < 8; ++i) {
            const int ra = 2 * i, rb = 2 * i + 1;
            const unsigned a0 = pk_bf16(o[0][ra], o[1][ra]), a1 = pk_bf16(o[2][ra], o[3][ra]);
            const unsigned c0 = pk_bf16(o[0][rb], o[1][rb]), c1 = pk_bf16(o[2][rb], o[3][rb]);
            const auto s0 = __builtin_amdgcn_permlane16_swap(a0, c0, false, false);
            const auto s1 = __builtin_amdgcn_permlane16_swap(a1, c1, false, false);
            st_sc1(rs, pb[ra >> 2] + 1024u * (unsigned)(ra & 3), v4u{s0[0], s1[0], s0[1], s1[1]});
        }
        STAMP(9);
        const unsigned fo = lane < 8 && v == 0 ? WS_FLAG + (unsigned)(lane * 8192 + b * 32) : OOB;
        asm volatile("s_waitcnt vmcnt(0)\n\ts_barrier" ::: "memory");
        __builtin_amdgcn_raw_buffer_store_b64(v2u{tag, ~tag}, rs, fo, 0, AUX_SC1);
        STAMP(10);
        if (ffn) {
            const int g2 = job >> 3, cb = job & 7;
            const rsrc_t r2 = mk_rsrc(w2, FF * 512 * 2);
            const unsigned hrow = 64u * g2 + 8u * (lane >> 3);
            const unsigned w2o = hrow < FF ? hrow * 1024u + 128u * cb + 16u * (lane & 7) : 0x7fffff00u;
            const unsigned hxo = WS_HX + (unsigned)(16 * (g2 >> 1) + (lane >> 5)) * 1024u + 512u * (g2 & 1) + 16u * (lane & 31);
            v4u fc[2][4], hx[8];
#pragma unroll
            for (int i = 0; i < 8; ++i) fc[i >> 2][i & 3] = __builtin_amdgcn_raw_buffer_load_b128(r2, w2o + 1024u * (i & 3), 4096 * (i >> 2), AUX_NT);
            PIN();
#pragma unroll 1
            for (unsigned spins = 0; spins < SPIN_CAP; ++spins) {
                CFENCE();
                bool ok = true;
#pragma unroll
                for (int i = 0; i < 8; ++i) hx[i] = ld_sc1(rs, hxo + 2048u * i);
#pragma unroll
                for (int i = 0; i < 8; ++i) ok &= tag_ok(hx[i], tag);
                if (__all(ok)) break;
            }
            const float h0 = ((__uint_as_float(hx[0].x) + __uint_as_float(hx[1].x)) + (__uint_as_float(hx[2].x) + __uint_as_float(hx[3].x))) + ((__uint_as_float(hx[4].x) + __uint_as_float(hx[5].x)) + (__uint_as_float(hx[6].x) + __uint_as_float(hx[7].x)));
            const float h1 = ((__uint_as_float(hx[0].z) + __uint_as_float(hx[1].z)) + (__uint_as_float(hx[2].z) + __uint_as_float(hx[3].z))) + ((__uint_as_float(hx[4].z) + __uint_as_float(hx[5].z)) + (__uint_as_float(hx[6].z) + __uint_as_float(hx[7].z)));
            const int hv = __float_as_int(silu(rs32(h0, h1)));
            f32x4 ha, hb;
#pragma unroll
            for (int i = 0; i < 4; ++i) {
                ha[i] = __int_as_float(__builtin_amdgcn_ds_bpermute(16 * (lane >> 3) + 4 * (i >> 1) + 128 * (i & 1), hv));
                hb[i] = __int_as_float(__builtin_amdgcn_ds_bpermute(16 * (lane >> 3) + 8 + 4 * (i >> 1) + 128 * (i & 1), hv));
            }
            f32x2 fo = gemv4(fc[0], ha) + gemv4(fc[1], hb);
            fo[0] += dpp<ROR8>(fo[0]); fo[1] += dpp<ROR8>(fo[1]);
            st_sc1(rs, (lane & 8) ? OOB : WS_ROW + (unsigned)(32 + g2) * 4096u + (unsigned)(32 * cb + 4 * (lane & 7) + (lane >> 4)) * 16u, v4u{__float_as_uint(fo[0]), tag, __float_as_uint(fo[1]), tag});
            STAMP(12);
        }
        STAMP_OUT();
        return;
    }
    if (wave == 8) {
        const char* src = (const char*)ckv + (size_t)b * 131072;
        const unsigned lane16 = 16u * lane;
        __builtin_amdgcn_s_waitcnt(0x0F70);
        BAR();
        if (late) lds_wait_idle(CTL(L_GO_WORD), L_GO);
        else __builtin_amdgcn_s_sleep(D_LE);
        STAMP(5);
#pragma unroll 1
        for (int g = 0; g < 8; ++g) {
#pragma unroll
            for (int u = 0; u < 4; ++u) dma4<WIN>(lane16 ^ (16u * u), src + 16384 * g + 4096 * u, L_CKV + 16384u * g + 4096u * u);
        }
        if (!late) __builtin_amdgcn_s_sleep(D_G);
        asm volatile("s_barrier\n\ts_barrier" ::: "memory");
        STAMP(9);
        asm volatile("s_waitcnt vmcnt(0)" ::: "memory");
        STAMP(10);
        asm volatile("s_barrier" ::: "memory");
        STAMP_OUT();
        return;
    }
    {
        asm volatile("" ::"s"(x), "s"(wq), "s"(wuk), "s"(wuv), "s"(wo), "s"(out), "s"(ws));
        unsigned ep;
        if (late) {
            const int row0 = 128 * s + 32 * w + 8 * (lane >> 4);
            const unsigned woff = (unsigned)row0 * 8192u + (unsigned)h * 256u + (unsigned)(lane & 15) * 16u;
            const v4u xv = *(const v4u*)(x + 8 * lane), xr = *(const v4u*)(x + row0);
            PIN();
            v4u a[2][4], u[8];
            const rsrc_t rq = mk_rsrc(wq, 4194304), rk = mk_rsrc(wuk, 4194304);
#pragma unroll
            for (int i = 0; i < 8; ++i) a[i >> 2][i & 3] = __builtin_amdgcn_raw_buffer_load_b128(rq, woff, i * 8192, AUX_NT);
            PIN();
            asm volatile("s_barrier" ::: "memory");
            PIN();
            ep = ld_epoch(ws);
            const unsigned tag = mk_tag(ep);
            float mean, rstd;
            ln_stats(xv, mean, rstd);
            f32x4 x0 = {(lo_f(xr.x) - mean) * rstd, (hi_f(xr.x) - mean) * rstd, (lo_f(xr.y) - mean) * rstd, (hi_f(xr.y) - mean) * rstd};
            f32x4 x1 = {(lo_f(xr.z) - mean) * rstd, (hi_f(xr.z) - mean) * rstd, (lo_f(xr.w) - mean) * rstd, (hi_f(xr.w) - mean) * rstd};
            asm volatile("" : "+v"(x0), "+v"(x1));
            PIN();
            __builtin_amdgcn_s_sleep(D_U);
#pragma unroll
            for (int i = 0; i < 8; ++i) u[i] = __builtin_amdgcn_raw_buffer_load_b128(rk, woff, i * 8192, AUX_NT);
            PIN();
            *(LAS f32x2*)(L + L_QP + 512 * w + 32 * (lane & 15) + 8 * (lane >> 4)) = gemv4(a[0], x0) + gemv4(a[1], x1);
            lds_add(CTL(C_QP), 1u);
            STAMP(1);
            PIN();
            float ul[8][8];
#pragma unroll
            for (int i = 0; i < 8; ++i)
#pragma unroll
                for (int k = 0; k < 4; ++k) { ul[i][2 * k] = lo_f(u[i][k]); ul[i][2 * k + 1] = hi_f(u[i][k]); }
#pragma unroll
            for (int i = 0; i < 8; ++i)
#pragma unroll
                for (int k = 0; k < 8; ++k) asm volatile("" : "+v"(ul[i][k]));
            lds_wait(CTL(C_QF), 1u);
            STAMP(2);
            const f32x4 qa = *(LAS f32x4*)(L + L_QF + 32 * (lane & 15)), qb = *(LAS f32x4*)(L + L_QF + 32 * (lane & 15) + 16);
            float t[8], r[4];
#pragma unroll
            for (int i = 0; i < 8; ++i) {
                float t0 = qa[0] * ul[i][0], t1 = qb[0] * ul[i][4];
#pragma unroll
                for (int k = 1; k < 4; ++k) { t0 += qa[k] * ul[i][k]; t1 += qb[k] * ul[i][4 + k]; }
                t[i] = t0 + t1;
            }
#pragma unroll
            for (int i = 0; i < 4; ++i) {
                const bool up = lane & 8;
                float v = (up ? t[i + 4] : t[i]) + dpp<ROR8>(up ? t[i] : t[i + 4]);
                v += dpp<QX1>(v); v += dpp<QX2>(v); v += dpp<HMIR>(v);
                r[i] = v * (0.08838834764831845f * 1.4426950408889634f);
            }
            const rsrc_t rs = mk_rsrc(ws, WS_END);
            st_sc1(rs, (lane & 7) == 0 ? WS_QA + (unsigned)(h * 128 + 32 * s + 8 * w + (lane >> 3)) * 16u : OOB, v4u{pk_bf16(r[0], r[1]), tag, pk_bf16(r[2], r[3]), tag});
            lds_add(CTL(C_QP), 1u);
            STAMP(3);
        } else {
            asm volatile("s_barrier" ::: "memory");
            ep = ld_epoch(ws);
        }
        const unsigned tag = mk_tag(ep);
        const rsrc_t rs = mk_rsrc(ws, WS_END);
        const unsigned r32 = (unsigned)(lane & 31), hh = (unsigned)(lane >> 5);
        v4u tv[4], tz[4];
        const rsrc_t rv = mk_rsrc(wuv, 4194304), ro = mk_rsrc(wo, 4194304);
        const unsigned vo = (unsigned)(128 * (s >> 1) + 8 * (s & 1) + 2 * w + (lane >> 5) + 16 * ((lane >> 4) & 1)) * 8192u + (unsigned)h * 256u + 16u * (lane & 15);
        const unsigned oo = (unsigned)(h * 128 + 32 * w + 4 * (lane >> 3)) * 1024u + 128u * s + 16u * (lane & 7);
#define TAIL_LOADS(COND) asm volatile("s_cmp_eq_u32 %15, 0\n\ts_cbranch_scc1 1f\n\t" \
                     "buffer_load_dwordx4 %0, %8, %10, 0 offen nt\n\tbuffer_load_dwordx4 %1, %8, %10, %12 offen nt\n\t" \
                     "buffer_load_dwordx4 %2, %8, %10, %13 offen nt\n\tbuffer_load_dwordx4 %3, %8, %10, %14 offen nt\n\t" \
                     "buffer_load_dwordx4 %4, %9, %11, 0 offen nt\n\tbuffer_load_dwordx4 %5, %9, %11, 0 offen offset:1024 nt\n\t" \
                     "buffer_load_dwordx4 %6, %9, %11, 0 offen offset:2048 nt\n\tbuffer_load_dwordx4 %7, %9, %11, 0 offen offset:3072 nt\n1:" \
                     : "+v"(tv[0]), "+v"(tv[1]), "+v"(tv[2]), "+v"(tv[3]), "+v"(tz[0]), "+v"(tz[1]), "+v"(tz[2]), "+v"(tz[3]) \
                     : "v"(vo), "v"(oo), "s"(rv), "s"(ro), "s"(32u * 8192u), "s"(64u * 8192u), "s"(96u * 8192u), "s"((unsigned)(COND)) : "scc", "memory")
#pragma unroll
        for (int i = 0; i < 4; ++i) { tv[i] = v4u{0u, 0u, 0u, 0u}; tz[i] = v4u{0u, 0u, 0u, 0u}; }
        BAR();
        {
            v4u g[8];
            asm volatile("buffer_load_dwordx4 %0, %8, %9, 0 offen sc1\n\tbuffer_load_dwordx4 %1, %8, %9, %10 offen sc1\n\t"
                         "buffer_load_dwordx4 %2, %8, %9, %11 offen sc1\n\tbuffer_load_dwordx4 %3, %8, %9, %12 offen sc1\n\t"
                         "buffer_load_dwordx4 %4, %8, %9, %13 offen sc1\n\tbuffer_load_dwordx4 %5, %8, %9, %14 offen sc1\n\t"
                         "buffer_load_dwordx4 %6, %8, %9, %15 offen sc1\n\tbuffer_load_dwordx4 %7, %8, %9, %16 offen sc1\n\ts_barrier"
                         : "=&v"(g[0]), "=&v"(g[1]), "=&v"(g[2]), "=&v"(g[3]), "=&v"(g[4]), "=&v"(g[5]), "=&v"(g[6]), "=&v"(g[7])
                         : "v"(goff), "s"(rs), "s"(8192u), "s"(16384u), "s"(24576u), "s"(32768u), "s"(40960u), "s"(49152u), "s"(57344u) : "memory");
            TAIL_LOADS((b >> 7) ^ 1);
            asm volatile("s_cmp_eq_u32 %8, 0\n\ts_cbranch_scc1 1f\n\ts_waitcnt vmcnt(0)\n1:\n\ts_waitcnt vmcnt(8)"
                         : "+v"(g[0]), "+v"(g[1]), "+v"(g[2]), "+v"(g[3]), "+v"(g[4]), "+v"(g[5]), "+v"(g[6]), "+v"(g[7]) : "s"(b >> 7) : "scc", "memory");
            gather_finish(rs, tag, g);
        }
        STAMP(4);
        unsigned a0[8], q0[8];
#pragma unroll
        for (int j = 0; j < 8; ++j) {
            const unsigned o = 1024u * r32 + 16u * ((2u * j + hh) ^ swz(r32 & 15u));
            a0[j] = L_CKV + 32768u * w + o; q0[j] = L_QS + o;
            asm volatile("" : "+v"(a0[j]), "+v"(q0[j]));
        }
        unsigned mxa = L_MX + 16u * r32, pa = L_P + 2048u * w + 16u * lane;
        asm volatile("" : "+v"(mxa), "+v"(pa));
        if (T_AT == 1) TAIL_LOADS(b >> 7);
        __builtin_amdgcn_s_setprio(2);
        BAR();
        STAMP(5);
        f32x16 sc;
#pragma unroll
        for (int i = 0; i < 16; ++i) sc[i] = 0.f;
        {
            bf16x8 af[32], qf[32];
#pragma unroll
            for (int ks = 0; ks < QK_PRE; ++ks) { af[ks] = *(LAS bf16x8*)(a0[ks & 7] + 256u * (ks >> 3)); qf[ks] = *(LAS bf16x8*)(q0[ks & 7] + 256u * (ks >> 3)); PIN(); }
#pragma unroll
            for (int ks = 0; ks < 32; ++ks) {
                sc = __builtin_amdgcn_mfma_f32_32x32x16_bf16(af[ks], qf[ks], sc, 0, 0, 0);
                PIN();
                const int kn = ks + QK_PRE;
                if (kn < 32) { af[kn] = *(LAS bf16x8*)(a0[kn & 7] + 256u * (kn >> 3)); qf[kn] = *(LAS bf16x8*)(q0[kn & 7] + 256u * (kn >> 3)); PIN(); }
            }
        }
        asm volatile("s_barrier\n\ts_nop 11" : "+v"(sc), "+v"(mxa), "+v"(pa) :: "memory");
        STAMP(6);
        float m = max3(max3(max3(sc[0], sc[1], sc[2]), max3(sc[3], sc[4], sc[5]), max3(sc[6], sc[7], sc[8])), max3(max3(sc[9], sc[10], sc[11]), max3(sc[12], sc[13], sc[14]), sc[15]), sc[15]);
        {
            const auto r = __builtin_amdgcn_permlane32_swap(__float_as_uint(m), __float_as_uint(m), false, false);
            *(LAS float*)(mxa + 4u * w) = max3(__uint_as_float(r[0]), __uint_as_float(r[1]), __uint_as_float(r[1]));
        }
        BAR();
        {
            const f32x4 mx = *(LAS f32x4*)mxa;
            m = ceilf(max3(max3(mx[0], mx[1], mx[2]), mx[3], mx[3]));
            float p[16];
#pragma unroll
            for (int i = 0; i < 16; ++i) p[i] = sc[i] - m;
            PIN();
#pragma unroll
            for (int i = 0; i < 8; ++i) p[i] = __builtin_amdgcn_exp2f(p[i]);
            PIN();
            *(LAS v4u*)pa = v4u{pk_bf16(p[0], p[1]), pk_bf16(p[2], p[3]), pk_bf16(p[4], p[5]), pk_bf16(p[6], p[7])};
            PIN();
#pragma unroll
            for (int i = 8; i < 16; ++i) p[i] = __builtin_amdgcn_exp2f(p[i]);
            PIN();
            *(LAS v4u*)(pa + 1024u) = v4u{pk_bf16(p[8], p[9]), pk_bf16(p[10], p[11]), pk_bf16(p[12], p[13]), pk_bf16(p[14], p[15])};
        }
        BAR();
        STAMP(7);
        if (w == 3) {
            float l0 = 0.f, l1 = 0.f;
#pragma unroll
            for (int kk = 0; kk < 8; ++kk) {
                const v4u pk = *(LAS v4u*)(L_P + 16u * lane + 1024u * kk);
#pragma unroll
                for (int k = 0; k < 4; ++k) { l0 += lo_f(pk[k]); l1 += hi_f(pk[k]); }
            }
            const float lt = rs32(l0 + l1, l0 + l1);
            st_sc1(rs, lane < 32 ? WS_ML + (unsigned)(b * 32 + 4 * (lane & 7) + (lane >> 3)) * 16u : OOB, v4u{__float_as_uint(m), tag, __float_as_uint(lt), tag});
            PIN();
        }
        __builtin_amdgcn_s_setprio(0);
        if (T_AT == 2) TAIL_LOADS(b >> 7);
        BAR();
        if (T_AT == 3) TAIL_LOADS(b >> 7);
        asm volatile("s_waitcnt vmcnt(0)" : "+v"(tv[0]), "+v"(tv[1]), "+v"(tv[2]), "+v"(tv[3]), "+v"(tz[0]), "+v"(tz[1]), "+v"(tz[2]), "+v"(tz[3]) :: "memory");
        STAMP(8);
        {
            const unsigned dbase = WS_PART + (unsigned)(tid & ~7) * 32768u + (unsigned)hs * 128u + 16u * (unsigned)(tid & 7);
            unsigned foff = WS_FLAG + (unsigned)(xg * 8192 + tid * 32), moff = WS_ML + (unsigned)(tid * 32 + 4 * xg + (h >> 3)) * 16u;
            v4u d[8];
#pragma unroll
            for (int n = 0; n < 8; ++n) d[n] = v4u{0u, 0u, 0u, 0u};
            float mj = 0.f, lj = 0.f;
            if (!late && w < 2) { for (int i = 0; i < D_E; ++i) __builtin_amdgcn_s_sleep(8); }
            STAMP(9);
            unsigned pend = 0xffu;
#pragma unroll 1
            for (unsigned spins = 0; spins < SPIN_CAP; ++spins) {
                CFENCE();
                const v4u g = ld_sc1(rs, moff);
                PIN();
                if (spins == 0u) __builtin_amdgcn_s_sleep(D_1);
                CFENCE();
                const v2u f = __builtin_amdgcn_raw_buffer_load_b64(rs, foff, 0, AUX_SC1);
                PIN();
                const bool ok = tag_ok(g, tag) & (moff != OOB);
                mj = ok ? __uint_as_float(g.x) : mj; lj = ok ? __uint_as_float(g.z) : lj;
                moff = ok ? OOB : moff;
                foff = ((f.x == tag) & (f.y == ~tag)) ? OOB : foff;
                unsigned long long miss = __builtin_amdgcn_ballot_w64(foff != OOB);
                miss |= miss >> 32; miss |= miss >> 16; miss |= miss >> 8;
                const unsigned rdy = pend & ~(unsigned)miss;
                pend &= (unsigned)miss;
                CFENCE();
#pragma unroll
                for (int n = 0; n < 8; ++n)
                    if ((rdy >> n) & 1u) d[n] = ld_sc1(rs, dbase + 32768u * n);
                if (pend == 0u && __all(moff == OOB)) break;
                if (rdy == 0u) __builtin_amdgcn_s_sleep(D_P);
            }
            STAMP(10);
            float wv[4][8];
#pragma unroll
            for (int i = 0; i < 4; ++i)
#pragma unroll
                for (int k = 0; k < 4; ++k) { wv[i][2 * k] = lo_f(tv[i][k]); wv[i][2 * k + 1] = hi_f(tv[i][k]); }
#pragma unroll
            for (int i = 0; i < 4; ++i)
#pragma unroll
                for (int k = 0; k < 8; ++k) asm volatile("" : "+v"(wv[i][k]));
            unsigned wh[8];
            float rl;
            {
                float M = fmaxf(mj, dpp<ROR8>(mj)); M = fmaxf(M, dpp<ROR4>(M)); M = fmaxf(M, dpp<ROR2>(M)); M = fmaxf(M, dpp<ROR1>(M)); M = mx32(mx16(M));
                const float Lw = wave_sum(__builtin_amdgcn_exp2f(mj - M) * lj);
                if (lane == 0) *(LAS f32x2*)(L + L_MLW + 8 * w) = f32x2{M, Lw};
                BAR();
                const f32x4 ml0 = *(LAS f32x4*)(L + L_MLW), ml1 = *(LAS f32x4*)(L + L_MLW + 16);
                const float Mg = fmaxf(fmaxf(ml0[0], ml0[2]), fmaxf(ml1[0], ml1[2]));
                rl = __builtin_amdgcn_rcpf((__builtin_amdgcn_exp2f(ml0[0] - Mg) * ml0[1] + __builtin_amdgcn_exp2f(ml0[2] - Mg) * ml0[3]) +
                                           (__builtin_amdgcn_exp2f(ml1[0] - Mg) * ml1[1] + __builtin_amdgcn_exp2f(ml1[2] - Mg) * ml1[3]));
                const int wt = (int)(__float_as_uint(__builtin_amdgcn_exp2f(mj - Mg)) & 0xffff0000u);
#pragma unroll
                for (int n = 0; n < 8; ++n) wh[n] = (unsigned)__builtin_amdgcn_ds_bpermute(4 * ((lane & 56) + n), wt);
#pragma unroll
                for (int n = 0; n < 8; ++n) asm volatile("" : "+v"(wh[n]));
                asm volatile("" : "+v"(rl));
                PIN();
            }
            float acc[8] = {0.f, 0.f, 0.f, 0.f, 0.f, 0.f, 0.f, 0.f};
#pragma unroll
            for (int n = 0; n < 8; ++n) {
                const unsigned wl = wh[n] >> 16;
#pragma unroll
                for (int k = 0; k < 4; ++k) { acc[2 * k] = dot2(d[n][k], wl, acc[2 * k]); acc[2 * k + 1] = dot2(d[n][k], wh[n], acc[2 * k + 1]); }
            }
            float r0 = rs16(rs32(acc[0], acc[4]), rs32(acc[2], acc[6])), r1 = rs16(rs32(acc[1], acc[5]), rs32(acc[3], acc[7]));
            r0 += dpp<ROR8>(r0); r1 += dpp<ROR8>(r1);
            *(LAS f32x2*)(L + L_RED + 256 * w + 32 * (lane & 7) + 8 * (lane >> 4)) = f32x2{r0, r1};
            BAR();
            STAMP(11);
            const unsigned ra = L_RED + 64u * w + 16u * (lane >> 4);
            const f32x4 cv = ((*(LAS f32x4*)ra + *(LAS f32x4*)(ra + 256u)) + (*(LAS f32x4*)(ra + 512u) + *(LAS f32x4*)(ra + 768u))) * rl;
            *(LAS f32x2*)(L + L_RED2 + 512 * w + 32 * (lane & 15) + 8 * (lane >> 4)) = gemv4(wv, cv);
            BAR();
            if (w == 3) {
                const unsigned rb = L_RED2 + 128u * (lane >> 4) + 16u * (lane & 7) + (lane & 8);
                const f32x2 cx = (*(LAS f32x2*)rb + *(LAS f32x2*)(rb + 512u)) + (*(LAS f32x2*)(rb + 1024u) + *(LAS f32x2*)(rb + 1536u));
                st_sc1(rs, WS_CX + (unsigned)(hs * 1024 + lane * 16), v4u{__float_as_uint(cx[0]), tag, __float_as_uint(cx[1]), tag});
            }
        }
        STAMP(12);
        {
            const unsigned off = WS_CX + (unsigned)((h * 8 + (lane & 7)) * 1024 + 256 * w + 16 * (lane >> 3));
            v4u g0, g1;
            float wz[4][8];
#pragma unroll
            for (int i = 0; i < 4; ++i)
#pragma unroll
                for (int k = 0; k < 4; ++k) { wz[i][2 * k] = lo_f(tz[i][k]); wz[i][2 * k + 1] = hi_f(tz[i][k]); }
#pragma unroll
            for (int i = 0; i < 4; ++i)
#pragma unroll
                for (int k = 0; k < 8; ++k) asm volatile("" : "+v"(wz[i][k]));
            __builtin_amdgcn_s_sleep(D_2 - 2);
#pragma unroll 1
            for (unsigned spins = 0; spins < SPIN_CAP; ++spins) {
                CFENCE();
                g0 = ld_sc1(rs, off); g1 = ld_sc1(rs, off + 128u);
                if (__all(tag_ok(g0, tag) & tag_ok(g1, tag))) break;
            }
            STAMP(13);
            f32x4 c4 = {__uint_as_float(g0.x), __uint_as_float(g0.z), __uint_as_float(g1.x), __uint_as_float(g1.z)};
#pragma unroll
            for (int i = 0; i < 4; ++i) { float t = c4[i]; t += dpp<QX1>(t); t += dpp<QX2>(t); t += dpp<HMIR>(t); c4[i] = t; }
            f32x2 o = gemv4(wz, c4);
            o[0] += dpp<ROR8>(o[0]); o[1] += dpp<ROR8>(o[1]);
            *(LAS f32x2*)(L + L_RED3 + 256 * w + 32 * (lane & 7) + 8 * (lane >> 4)) = o;
            BAR();
        }
        if (w == 3) {
            const unsigned rc = L_RED3 + 8u * (lane & 31);
            const f32x2 o = (*(LAS f32x2*)rc + *(LAS f32x2*)(rc + 256u)) + (*(LAS f32x2*)(rc + 512u) + *(LAS f32x2*)(rc + 768u));
            st_sc1(rs, lane < 32 ? WS_ROW + (unsigned)(h * 4096 + (32 * s + lane) * 16) : OOB, v4u{__float_as_uint(o[0]), tag, __float_as_uint(o[1]), tag});
        }
        if (w == 0) {
            const unsigned cp = (unsigned)(32 * xg + (b >> 3));
            const unsigned xo = *(const unsigned*)(x + 2 * cp);
            unsigned off = lane < 54 ? WS_ROW + (unsigned)lane * 4096u + cp * 16u : OOB;
            float s0 = 0.f, s1 = 0.f;
            STAMP(14);
            __builtin_amdgcn_s_sleep(D_3);
#pragma unroll 1
            for (unsigned spins = 0; spins < SPIN_CAP; ++spins) {
                CFENCE();
                const v4u g = ld_sc1(rs, off);
                const bool ok = tag_ok(g, tag) & (off != OOB);
                s0 = ok ? __uint_as_float(g.x) : s0; s1 = ok ? __uint_as_float(g.z) : s1;
                off = ok ? OOB : off;
                if (__all(off == OOB)) break;
            }
            float ss = rs16(s0, s1);
            ss += dpp<ROR8>(ss); ss += dpp<ROR4>(ss); ss += dpp<ROR2>(ss); ss += dpp<ROR1>(ss);
            const float r0 = lo_f(xo) + rdlane(ss, 0) * silu(rdlane(ss, 32)), r1 = hi_f(xo) + rdlane(ss, 16) * silu(rdlane(ss, 48));
            if (lane == 0) {
                *(unsigned*)(out + 2 * cp) = pk_bf16(r0, r1);
                if (b == 0) __hip_atomic_store((GAS unsigned*)(ws + WS_EPOCH), ep + 1u, __ATOMIC_RELAXED, __HIP_MEMORY_SCOPE_AGENT);
            }
        }
        STAMP(15);
        STAMP_OUT();
    }
}
}

extern "C" void kernel_launch(void* const* d_in, const int* in_sizes, int n_in, void* d_out, int out_size, void* d_ws, size_t ws_size, hipStream_t stream) {
    (void)in_sizes; (void)n_in; (void)out_size; (void)ws_size;
    mla_decode_kernel<<<NWG, NT, 0, stream>>>((const u16*)d_in[0], (const u16*)d_in[1], (const u16*)d_in[2], (const u16*)d_in[3], (const u16*)d_in[4],
                                              (const u16*)d_in[5], (const u16*)d_in[7], (const u16*)d_in[8], (u16*)d_out, (char*)d_ws);
}
```
